```python
import torch

def _softmax(x, axis=-1):
    return torch.softmax(x, dim=axis)
def _gelu(x):
    return torch.nn.functional.gelu(x, approximate="tanh")
def _silu(x):
    return torch.nn.functional.silu(x)
def _sigmoid(x):
    return torch.sigmoid(x)
def _layernorm(x, axis=-1, eps=1e-5):
    m = x.mean(dim=axis, keepdim=True)
    v = x.var(dim=axis, keepdim=True, unbiased=False)
    return (x - m) / torch.sqrt(v + eps)
def _scatter_add(tgt, idx, src, axis=0):
    return tgt.clone().index_add_(axis, idx, src)
def _segment_sum(src, seg, n):
    out = torch.zeros((n,) + src.shape[1:], dtype=src.dtype)
    return out.index_add_(0, seg, src)
def _pad(x, pw):
    flat = tuple(v for pair in reversed(pw) for v in pair)
    return torch.nn.functional.pad(x, flat)

kernel_name = 'recurrent_scan__ssm_conv_s4__dense_activation_gelu__prompt'

def setup_inputs():

    _w = (lambda a: (lambda t: t.to(torch.bfloat16) if t.is_floating_point() else t)(torch.as_tensor(a)))
    def _A_setup():
        B, L, D, N = 1, 2048, 640, 192
        A_re = -(lambda _x: torch.exp(torch.as_tensor(_x)))((lambda *s: torch.randn(*s, dtype=torch.bfloat16) * 3.2)(D, N))
        A_im = torch.pi * (lambda *s: torch.randn(*s, dtype=torch.bfloat16) * 0.2)(D, N)
        B_re = (lambda *s: torch.randn(*s, dtype=torch.bfloat16) * 0.2)(D, N)
        B_im = (lambda *s: torch.randn(*s, dtype=torch.bfloat16) * 0.2)(D, N)
        C_re = (lambda *s: torch.randn(*s, dtype=torch.bfloat16) * 0.2)(D, N)
        C_im = (lambda *s: torch.randn(*s, dtype=torch.bfloat16) * 0.2)(D, N)
        log_dt = (lambda *s: torch.randn(*s, dtype=torch.bfloat16) * 0.2)(D) * 0.5 - 4.0
        Dskip = (lambda *s: torch.randn(*s, dtype=torch.bfloat16) * 0.2)(D)
        return {
            "u": _w((lambda *s: torch.randn(*s, dtype=torch.bfloat16) * 0.05)(B, L, D)),
            "A_re": _w(A_re),
            "A_im": _w(A_im),
            "B_re": _w(B_re),
            "B_im": _w(B_im),
            "C_re": _w(C_re),
            "C_im": _w(C_im),
            "log_dt": _w(log_dt),
            "Dskip": _w(Dskip),
        }
    _A_in = _A_setup()

    def _B_setup():
        B, S, D, F = 1, 2048, 640, 1536
        return {
            "x": _w((lambda *s: torch.randn(*s, dtype=torch.bfloat16) * 0.05)(B, S, D)),
            "w1": _w((lambda *s: torch.randn(*s, dtype=torch.bfloat16) * 0.8)(D, F) * (D ** -0.5)),
            "b1": _w((lambda *s: torch.randn(*s, dtype=torch.bfloat16) * 0.1)(F) * 0.01),
            "w2": _w((lambda *s: torch.randn(*s, dtype=torch.bfloat16) * 0.1)(F, D) * (F ** -0.5)),
            "b2": _w((lambda *s: torch.randn(*s, dtype=torch.bfloat16) * 0.1)(D) * 0.01),
        }
    _B_in = _B_setup()

    return {
        "A_u": _A_in["u"],
        "A_A_re": _A_in["A_re"],
        "A_A_im": _A_in["A_im"],
        "A_B_re": _A_in["B_re"],
        "A_B_im": _A_in["B_im"],
        "A_C_re": _A_in["C_re"],
        "A_C_im": _A_in["C_im"],
        "A_log_dt": _A_in["log_dt"],
        "A_Dskip": _A_in["Dskip"],
        "B_x": _B_in["x"],
        "B_w1": _B_in["w1"],
        "B_b1": _B_in["b1"],
        "B_w2": _B_in["w2"],
        "B_b2": _B_in["b2"],
    }

def reference(A_u, A_A_re, A_A_im, A_B_re, A_B_im, A_C_re, A_C_im, A_log_dt, A_Dskip, B_x, B_w1, B_b1, B_w2, B_b2):
    def _A_ref(u, A_re, A_im, B_re, B_im, C_re, C_im, log_dt, Dskip):
        Bsz, L, D = u.shape
        dt = torch.exp(log_dt)[:, None]
        A = A_re + 1j * A_im
        Bc = B_re + 1j * B_im
        Cc = C_re + 1j * C_im
        logdA = dt * A
        dB = dt * Bc
        l = (lambda x, dt: x.to(dt))(torch.arange(L), A_re.dtype)
        P = torch.exp(logdA[:, :, None] * l[None, None, :])
        K = torch.einsum("dn,dnl,dn->dl", Cc, P, dB).real
        L2 = 2 * L
        Kp = _pad(K, ((0, 0), (0, L)))
        up = (lambda x, *d: x.permute(*d))(u, 0, 2, 1)
        up = _pad(up, ((0, 0), (0, 0), (0, L)))
        Kf = (lambda x, axes=(-2,-1): torch.fft.rfft2(x.float(), dim=axes))(Kp[:, None, :], axes=(-2, -1))
        Uf = (lambda x, axes=(-2,-1): torch.fft.rfft2(x.float(), dim=axes))(up[:, :, None, :], axes=(-2, -1))
        Yf = Uf * Kf[None, :, :, :]
        y = (lambda x, s, axes=(-2,-1): torch.fft.irfft2(x, s=s, dim=axes).to(torch.bfloat16))(Yf, (1, L2), axes=(-2, -1))[:, :, 0, :L]
        y = (lambda x, *d: x.permute(*d))(y, 0, 2, 1) + u * Dskip[None, None, :]
        return y

    def _B_ref(x, w1, b1, w2, b2):
        h = torch.einsum("bsd,df->bsf", x, w1) + b1
        h = _gelu(h)
        return torch.einsum("bsf,fd->bsd", h, w2) + b2

    x = A_u
    xn = _layernorm(x, axis=-1)
    s = _A_ref(xn, A_A_re, A_A_im, A_B_re, A_B_im, A_C_re, A_C_im, A_log_dt, A_Dskip)
    g = _silu(_B_ref(xn, B_w1, B_b1, B_w2, B_b2))
    return x + s * g
```

```cpp
#include <hip/hip_runtime.h>
#include <hip/hip_bf16.h>
#include <stdint.h>

typedef unsigned short u16;
#define L_ 2048
#define D_ 640
#define N_ 192
#define F_ 1536

__device__ __forceinline__ float bf2f(u16 v){ return __uint_as_float(((unsigned)v)<<16); }
__device__ __forceinline__ u16 f2bf(float f){ unsigned u=__float_as_uint(f); u += 0x7fffu + ((u>>16)&1u); return (u16)(u>>16); }
__device__ __forceinline__ float rbf(float f){ return bf2f(f2bf(f)); }

__global__ void ln_k(const u16* x, u16* xn){
  int t = blockIdx.x; int tid = threadIdx.x;
  float v[10]; float s=0;
  for(int i=0;i<10;i++){ v[i]=bf2f(x[t*D_+tid+64*i]); s+=v[i]; }
  for(int o=32;o>0;o>>=1) s+=__shfl_xor(s,o);
  float mean=s/D_;
  float q=0; for(int i=0;i<10;i++){ float d=v[i]-mean; q+=d*d; }
  for(int o=32;o>0;o>>=1) q+=__shfl_xor(q,o);
  float var=rbf(q/D_); float m=rbf(mean);
  float sd=rbf(sqrtf(rbf(var+1e-5f)));
  for(int i=0;i<10;i++){ xn[t*D_+tid+64*i]=f2bf(rbf(v[i]-m)/sd); }
}

__global__ void kgen_k(const u16* Are,const u16* Aim,const u16* Bre,const u16* Bim,const u16* Cre,const u16* Cim,const u16* logdt,float* K){
  int d=blockIdx.y; int l=blockIdx.x*256+threadIdx.x;
  float dt=rbf(expf(bf2f(logdt[d])));
  float lf=rbf((float)l);
  float acc=0;
  for(int n=0;n<N_;n++){
    int i=d*N_+n;
    float ar=dt*bf2f(Are[i]), ai=dt*bf2f(Aim[i]);
    float br=dt*bf2f(Bre[i]), bi=dt*bf2f(Bim[i]);
    float cr=bf2f(Cre[i]), ci=bf2f(Cim[i]);
    float e=expf(ar*lf); float sn,cs; sincosf(ai*lf,&sn,&cs);
    float pr=e*cs, pi=e*sn;
    float wr=cr*br-ci*bi, wi=cr*bi+ci*br;
    acc+=wr*pr-wi*pi;
  }
  K[d*L_+l]=acc;
}

__global__ void conv_k(const u16* xn,const float* K,const u16* Dskip,u16* s){
  int d=blockIdx.x*64+threadIdx.x; int t=blockIdx.y;
  float acc=0;
  for(int l=0;l<=t;l++) acc+=K[d*L_+l]*bf2f(xn[(t-l)*D_+d]);
  float u=bf2f(xn[t*D_+d]);
  s[t*D_+d]=f2bf(rbf(acc)+rbf(u*bf2f(Dskip[d])));
}

__global__ void g1_k(const u16* xn,const u16* w1,const u16* b1,u16* h){
  int f=blockIdx.x*64+threadIdx.x; int t=blockIdx.y;
  float acc=0;
  for(int k=0;k<D_;k++) acc+=bf2f(xn[t*D_+k])*bf2f(w1[k*F_+f]);
  float v=rbf(rbf(acc)+bf2f(b1[f]));
  float g=0.5f*v*(1.f+tanhf(0.7978845608028654f*(v+0.044715f*v*v*v)));
  h[t*F_+f]=f2bf(g);
}
__global__ void g2_k(const u16* h,const u16* w2,const u16* b2,const u16* x,const u16* s,u16* out){
  int d=blockIdx.x*64+threadIdx.x; int t=blockIdx.y;
  float acc=0;
  for(int k=0;k<F_;k++) acc+=bf2f(h[t*F_+k])*bf2f(w2[k*D_+d]);
  float v=rbf(rbf(acc)+bf2f(b2[d]));
  float g=rbf(v/(1.f+expf(-v)));
  float sg=rbf(bf2f(s[t*D_+d])*g);
  out[t*D_+d]=f2bf(bf2f(x[t*D_+d])+sg);
}

extern "C" void kernel_launch(void* const* d_in, const int* in_sizes, int n_in,
                              void* d_out, int out_size, void* d_ws, size_t ws_size,
                              hipStream_t stream) {
  const u16* x=(const u16*)d_in[0];
  char* ws=(char*)d_ws;
  u16* xn=(u16*)ws; ws+=L_*D_*2;
  u16* s=(u16*)ws; ws+=L_*D_*2;
  u16* h=(u16*)ws; ws+=L_*F_*2;
  float* K=(float*)ws;
  ln_k<<<L_,64,0,stream>>>(x,xn);
  kgen_k<<<dim3(L_/256,D_),256,0,stream>>>((const u16*)d_in[1],(const u16*)d_in[2],(const u16*)d_in[3],(const u16*)d_in[4],(const u16*)d_in[5],(const u16*)d_in[6],(const u16*)d_in[7],K);
  conv_k<<<dim3(D_/64,L_),64,0,stream>>>(xn,K,(const u16*)d_in[8],s);
  g1_k<<<dim3(F_/64,L_),64,0,stream>>>(xn,(const u16*)d_in[10],(const u16*)d_in[11],h);
  g2_k<<<dim3(D_/64,L_),64,0,stream>>>(h,(const u16*)d_in[12],(const u16*)d_in[13],x,s,(u16*)d_out);
}
```

```cpp
#include <hip/hip_runtime.h>
#include <stdint.h>

typedef unsigned short u16;
typedef unsigned int u32;
typedef unsigned long long u64;
typedef short s16x4 __attribute__((ext_vector_type(4)));
typedef short s16x8 __attribute__((ext_vector_type(8)));
typedef _Float16 h16x8 __attribute__((ext_vector_type(8)));
typedef _Float16 h16x2 __attribute__((ext_vector_type(2)));
typedef __bf16 b16x2 __attribute__((ext_vector_type(2)));
typedef __bf16 b16x8 __attribute__((ext_vector_type(8)));
typedef float f32x2 __attribute__((ext_vector_type(2)));
typedef float f32x4 __attribute__((ext_vector_type(4)));
typedef float f32x16 __attribute__((ext_vector_type(16)));
typedef u32 u32x4 __attribute__((ext_vector_type(4)));
typedef u32 u32x2 __attribute__((ext_vector_type(2)));
#define LDSP(T) __attribute__((address_space(3))) T*
#define DEV __device__ __forceinline__

constexpr u32 WS_EPOCH = 0;
constexpr u32 WS_F0 = 1024, WS_F1 = 2048, WS_F2 = 3072;
constexpr u32 WS_STATS = 4096;
constexpr u32 WS_UT = 32768;
constexpr u32 WS_W2R = WS_UT + 256 * 640 * 16;
constexpr u32 WS_HF = WS_W2R + 8 * 48 * 5 * 1024;
constexpr u32 WS_ST = WS_HF + 32 * 48 * 4 * 1024;
constexpr u32 WS_END = WS_ST + 640 * 2048 * 2;
constexpr u32 WS_DBG = WS_END;

constexpr u32 SLOT = 28672, XOFF = 12288;
constexpr u32 AREG = 4 * SLOT;
constexpr u32 CHSZ = 16384;
constexpr u32 TABS = 2624;
constexpr u32 P3O = 4 * TABS;
constexpr u32 UPO = P3O + 512;
constexpr u32 SCR_ROW = 272, SCR_COL = 25 * SCR_ROW;
constexpr u32 L_CS = AREG + 15616;
constexpr u32 L_SYNC = AREG + CHSZ + 15616;
constexpr u32 SY_G = 0, SY_OK0 = 4, SY_A = 8, SY_CS = 12, SY_H = 16, SY_S = 20, SY_A2 = 24, SY_GD = 28, SY_TAG = 32;
constexpr u32 L_RED = 0;
constexpr u32 L_SIMG = 32768;
constexpr u32 PROW = 336, PSZ = 64 * PROW;
constexpr u32 L_S2 = AREG;
constexpr u32 S2ROW = 176;
constexpr u32 L_B2 = AREG + 12288;
constexpr u32 LDS_BYTES = 163840;
constexpr int SPIN_MAX = 1 << 22;

struct Args {
  const u16 *x, *are, *aim, *bre, *bim, *cre, *cim, *ldt, *dsk, *w1, *b1, *w2, *b2;
  u16* out;
  char* ws;
};

DEV float bf_lo(u32 v) { return __uint_as_float(v << 16); }
DEV float bf_hi(u32 v) { return __uint_as_float(v & 0xffff0000u); }
DEV u32 pk_bf(float a, float b) { return __builtin_bit_cast(u32, __builtin_convertvector((f32x2){a, b}, b16x2)); }
DEV u32 pk_h(float a, float b) { return __builtin_bit_cast(u32, __builtin_convertvector((f32x2){a, b}, h16x2)); }
DEV float rbf(float a) { return bf_lo(pk_bf(a, a)); }
DEV u32 rfl(u32 v) { return __builtin_amdgcn_readfirstlane(v); }
template <class T> DEV T ldsr(u32 a) { return *(LDSP(T))(size_t)(a); }
template <class T> DEV void ldsw(u32 a, T v) { *(LDSP(T))(size_t)(a) = v; }
DEV s16x4 ldstr(u32 a) { return __builtin_amdgcn_ds_read_tr16_b64_v4i16((LDSP(s16x4))(size_t)(a)); }
DEV u32 lds_ld(u32 a) { return __hip_atomic_load((LDSP(u32))(size_t)(a), __ATOMIC_RELAXED, __HIP_MEMORY_SCOPE_WORKGROUP); }
DEV u32 lds_add(u32 a) { return __hip_atomic_fetch_add((LDSP(u32))(size_t)(a), 1u, __ATOMIC_RELAXED, __HIP_MEMORY_SCOPE_WORKGROUP); }
#define FENCE() asm volatile("" ::: "memory")
#define WAITV(n) asm volatile("s_waitcnt vmcnt(" #n ")" ::: "memory")
#define WAITL0() asm volatile("s_waitcnt lgkmcnt(0)" ::: "memory")
DEV void lds_arrive(u32 a, int lane) { FENCE(); if (lane == 0) (void)lds_add(a); FENCE(); }
DEV void lds_wait(u32 a, u32 n) {
  FENCE();
  for (int it = 0; it < SPIN_MAX; ++it) {
    if (rfl(lds_ld(a)) >= n) break;
    __builtin_amdgcn_s_sleep(1);
  }
  FENCE();
}
DEV void dma16(u32 voff, const void* sbase, u32 lds) {
  asm volatile("s_mov_b32 m0, %2\n\ts_nop 0\n\tglobal_load_lds_dwordx4 %0, %1" ::"v"(voff), "s"(sbase), "s"(lds) : "memory");
}
DEV u32 cmul16(u32 a, u32 b) {
  u32 t, r;
  asm("v_pk_mul_f16 %0, %1, %2 op_sel:[1,1] op_sel_hi:[1,0] neg_lo:[1,0]" : "=v"(t) : "v"(a), "v"(b));
  asm("v_pk_fma_f16 %0, %1, %2, %3 op_sel:[0,0,0] op_sel_hi:[0,1,1]" : "=v"(r) : "v"(a), "v"(b), "v"(t));
  return r;
}
DEV f32x4 selv4(bool c, f32x4 x, f32x4 y) { asm("" : "+v"(x)); asm("" : "+v"(y)); return c ? x : y; }
struct cf { float x, y; };
DEV cf cmul(cf a, cf b) { return {__builtin_fmaf(a.x, b.x, -(a.y * b.y)), __builtin_fmaf(a.x, b.y, a.y * b.x)}; }
DEV cf csq(cf a) { return {__builtin_fmaf(a.x, a.x, -(a.y * a.y)), (a.x + a.x) * a.y}; }
DEV cf cexp_l(float lr, float rev, float l) {
  float m = __builtin_amdgcn_exp2f(lr * l), p = __builtin_amdgcn_fractf(rev * l);
  return {m * __builtin_amdgcn_cosf(p), m * __builtin_amdgcn_sinf(p)};
}
#define MF32B(a, b, c) __builtin_amdgcn_mfma_f32_32x32x16_bf16(__builtin_bit_cast(b16x8, a), __builtin_bit_cast(b16x8, b), c, 0, 0, 0)
#define MF16B(a, b, c) __builtin_amdgcn_mfma_f32_16x16x32_bf16(__builtin_bit_cast(b16x8, a), __builtin_bit_cast(b16x8, b), c, 0, 0, 0)
#define MF32H(a, b, c) __builtin_amdgcn_mfma_f32_32x32x16_f16(__builtin_bit_cast(h16x8, a), __builtin_bit_cast(h16x8, b), c, 0, 0, 0)
#define MF16H(a, b, c) __builtin_amdgcn_mfma_f32_16x16x32_f16(__builtin_bit_cast(h16x8, a), __builtin_bit_cast(h16x8, b), c, 0, 0, 0)
#define SB() __builtin_amdgcn_sched_barrier(0)

#define STAMP(i) do {} while (0)

typedef __amdgpu_buffer_rsrc_t rsrc_t;
DEV u32x4 bld(rsrc_t r, u32 v, u32 s) { return __builtin_amdgcn_raw_buffer_load_b128(r, v, s, 16); }
DEV void bst(u32x4 d, rsrc_t r, u32 v, u32 s) { __builtin_amdgcn_raw_buffer_store_b128(d, r, v + s, 0, 16); }

DEV void poll256(rsrc_t r, u32 off, u32 tag, int lane) {
  for (int it = 0; it < SPIN_MAX; ++it) {
    u32x4 f = bld(r, lane * 16, off);
    bool ok = f[0] == tag && f[1] == tag && f[2] == tag && f[3] == tag;
    if (__builtin_amdgcn_ballot_w64(!ok) == 0) break;
    __builtin_amdgcn_s_sleep(2);
  }
  FENCE();
}

u32 __device__ __forceinline__ slot2d(u32 s) { return (s & ~127u) + 2 * (s & 63) + ((s >> 6) & 1); }

__global__ __launch_bounds__(512, 2) void s4_block(Args a) {
  extern __shared__ __attribute__((aligned(16))) char smem[];
  const int tid = threadIdx.x, lane = tid & 63;
  const int wave = rfl(tid >> 6);
  const int b = blockIdx.x, xcd = b & 7, ii = b >> 3;
  const rsrc_t rw = __builtin_amdgcn_make_buffer_rsrc((void*)a.ws, 0, 0x7fffffff, 0x00020000);
  const int rt = 8 * (xcd & 3) + 4 * (xcd >> 2) + (ii & 3), ct = ii >> 2;
  STAMP(0);
  const int it0 = tid, it1 = 512 + (tid & 127);
  u32x4 xi0, xi1;

  if (wave < 4) {
    const int w = wave;
    const int p = 4 * (xcd & 3) + (ii & 3), j = 8 * (xcd >> 2) + (ii >> 2);
    u32 voffW[3], voffX[4];
#pragma unroll
    for (int u = 0; u < 3; ++u) {
      u32 off = 1024 * (3 * w + u) + 16 * lane;
      u32 row = off / 192;
      voffW[u] = row * 3072 + (off - row * 192);
    }
#pragma unroll
    for (int q = 0; q < 4; ++q) voffX[q] = (8 * q + (lane >> 3)) * 1280 + (((lane & 7) ^ ((4 * q + (lane >> 4)) & 7)) * 16);
    const char* gW = (const char*)a.w1 + j * 192;
    const char* gX = (const char*)a.x + (128 * p + 32 * w) * 1280;
    auto issue = [&](int c) {
      const u32 s = (c & 3) * SLOT;
#pragma unroll
      for (int q = 0; q < 4; ++q) dma16(voffX[q], gX + 128 * c, s + XOFF + 4096 * w + 1024 * q);
#pragma unroll
      for (int u = 0; u < 3; ++u) dma16(voffW[u], gW + c * 64 * 3072, s + 1024 * (3 * w + u));
    };
    issue(0); issue(1); issue(2); issue(3);
    __builtin_amdgcn_s_barrier();
    FENCE();
    const u32 tag = rfl(lds_ld(L_SYNC + SY_TAG));
    STAMP(1);

    const int wt = w == 3 ? 0 : w;
    u32 ab[3], bb[4];
    {
      const u32 G = lane >> 4, q = (lane >> 2) & 3, pp = lane & 3;
      const u32 base = (8 * (G >> 1) + q) * 192 + (16 * (G & 1) + 4 * pp) * 2;
#pragma unroll
      for (int i = 0; i < 3; ++i) ab[i] = base + 64 * ((wt + i) % 3);
      const u32 t = lane & 31, g = lane >> 5;
#pragma unroll
      for (int k = 0; k < 4; ++k) bb[k] = XOFF + 4096 * w + t * 128 + (((2 * k + g) ^ ((t >> 1) & 7)) * 16);
    }
    const u32 selv = ((lane == 0 || lane == 32 || lane == 20 || lane == 52) ? 0x3f803f80u : 0u);
    const u32x4 sel = {selv, selv, selv, selv};
    f32x16 acc[3] = {};
    f32x4 csacc = {};
    s16x8 fa[2][3];
    u32x4 fb[2];
    auto rd = [&](int s) {
      const u32 so = ((s >> 2) & 3) * SLOT, k = s & 3;
#pragma unroll
      for (int i = 0; i < 3; ++i) {
        s16x4 lo = ldstr(ab[i] + so + (16 * k) * 192), hi = ldstr(ab[i] + so + (16 * k + 4) * 192);
        fa[s & 1][i] = __builtin_shufflevector(lo, hi, 0, 1, 2, 3, 4, 5, 6, 7);
      }
      fb[s & 1] = ldsr<u32x4>(bb[k] + so);
    };
    auto mm = [&](int s) {
#pragma unroll
      for (int i = 0; i < 3; ++i) acc[i] = MF32B(fa[s & 1][i], fb[s & 1], acc[i]);
      csacc = MF16B(sel, fa[s & 1][0], csacc);
    };
    WAITV(21);
    lds_arrive(L_SYNC + SY_G, lane);
    lds_wait(L_SYNC + SY_G, 4);
    rd(0);
    u32 b1v[3];
    u32x2 gran;
#pragma unroll
    for (int c = 0; c < 10; ++c) {
      SB(); rd(4 * c + 1); SB(); mm(4 * c); SB();
      rd(4 * c + 2); SB(); mm(4 * c + 1); SB();
      rd(4 * c + 3); SB();
      if (c < 9) {
        if (c <= 6) WAITV(14); else if (c == 7) WAITV(7); else WAITV(0);
        lds_arrive(L_SYNC + SY_G, lane);
      }
      SB();
      acc[0] = MF32B(fa[0][0], fb[0], acc[0]);
      acc[1] = MF32B(fa[0][1], fb[0], acc[1]);
      SB();
      u32 cv = 0;
      if (c < 9) cv = lds_ld(L_SYNC + SY_G);
      SB();
      acc[2] = MF32B(fa[0][2], fb[0], acc[2]);
      csacc = MF16B(sel, fa[0][0], csacc);
      SB();
      if (c < 9) {
        if (rfl(cv) < 4 * (c + 2)) lds_wait(L_SYNC + SY_G, 4 * (c + 2));
        FENCE();
        if (c + 4 <= 9) issue(c + 4);
        rd(4 * c + 4);
      }
      SB(); mm(4 * c + 3); SB();
    }
    STAMP(2);
    {
      float cs = csacc[0];
      u32 hi = pk_bf(cs, cs) & 0xffffu;
      u32 lo = pk_bf(cs - bf_lo(hi), 0.f);
      if (w < 3 && lane < 32) ldsw<u32>(L_CS + 128 * w + 4 * lane, hi | (lo << 16));
      lds_arrive(L_SYNC + SY_CS, lane);
    }
#pragma unroll
    for (int i = 0; i < 3; ++i) b1v[i] = a.b1[96 * j + 32 * ((wt + i) % 3) + (lane & 31)];
    for (int it = 0; it < SPIN_MAX; ++it) {
      gran = __builtin_amdgcn_raw_buffer_load_b64(rw, (128 * p + 32 * w + (lane & 31)) * 8, WS_STATS, 16);
      if (__builtin_amdgcn_ballot_w64(gran[1] != tag) == 0) break;
      __builtin_amdgcn_s_sleep(2);
    }
    lds_wait(L_SYNC + SY_CS, 4);
    const float rstd = __builtin_amdgcn_rcpf(bf_hi(gran[0]));
    {
      const bool g0 = lane < 32;
      u32 nm = (gran[0] & 0xffffu) ^ 0x8000u;
      u32x4 fbx = {g0 ? (nm | (nm << 16)) : 0u, g0 ? (gran[0] >> 16) : 0u, 0u, 0u};
#pragma unroll
      for (int i = 0; i < 3; ++i) {
        u32 cs = ldsr<u32>(L_CS + 128 * ((wt + i) % 3) + 4 * (lane & 31));
        u32x4 fax = {g0 ? cs : 0u, g0 ? b1v[i] : 0u, 0u, 0u};
        acc[i] = MF32B(fax, fbx, acc[i]);
      }
    }
    STAMP(3);
    {
      const u32 vo = (2 * (w & 1) + ((lane >> 4) & 1)) * 1024 + ((lane & 15) + 16 * (lane >> 5)) * 16;
#pragma unroll
      for (int i = 0; i < 3; ++i) {
        const u32 so = WS_HF + ((2 * p + (w >> 1)) * 48 + 3 * j + (wt + i) % 3) * 4096;
#pragma unroll
        for (int hh = 0; hh < 2; ++hh) {
          float hv[8];
#pragma unroll
          for (int e = 0; e < 8; ++e) {
            float v = acc[i][8 * hh + e] * rstd;
            float u = __builtin_fmaf(v * v, -0.10294324f, -2.3022082f);
            float ex = __builtin_amdgcn_exp2f(u * v);
            hv[e] = v * __builtin_amdgcn_rcpf(1.f + ex);
          }
          u32x4 o = {pk_bf(hv[0], hv[1]), pk_bf(hv[2], hv[3]), pk_bf(hv[4], hv[5]), pk_bf(hv[6], hv[7])};
          bst(o, rw, vo + hh * 512, so);
        }
      }
    }
    WAITV(0);
    {
      u32 old = 0;
      if (lane == 0) old = lds_add(L_SYNC + SY_H);
      old = rfl(old);
      if (old == 3 && lane == 0) __builtin_amdgcn_raw_buffer_store_b32(tag, rw, (p * 16 + j) * 4, WS_F1, 16);
    }
    STAMP(4);
    xi0 = *(const u32x4*)((const char*)a.x + (64 * rt + it0 / 10) * 1280 + ct * 160 + (it0 % 10) * 16);
    xi1 = *(const u32x4*)((const char*)a.x + (64 * rt + it1 / 10) * 1280 + ct * 160 + (it1 % 10) * 16);
    lds_wait(L_SYNC + SY_OK0, 1);
    const u32 wbase = WS_W2R + (ct * 48 + 12 * w) * 5120, hbase = WS_HF + (rt * 48 + 12 * w) * 4096;
    u32x4 wf[12][5], hf[12][4];
#pragma unroll
    for (int k = 0; k < 4; ++k)
#pragma unroll
      for (int n = 0; n < 5; ++n) wf[k][n] = bld(rw, lane * 16, wbase + (k * 5 + n) * 1024);
    for (int it = 0; it < SPIN_MAX; ++it) {
      u32 f = __builtin_amdgcn_raw_buffer_load_b32(rw, ((rt >> 1) * 16 + 4 * w + (lane & 3)) * 4, WS_F1, 16);
      if (__builtin_amdgcn_ballot_w64(f != tag) == 0) break;
      __builtin_amdgcn_s_sleep(2);
    }
    FENCE();
    STAMP(5);
#pragma unroll
    for (int k = 0; k < 4; ++k)
#pragma unroll
      for (int m = 0; m < 4; ++m) hf[k][m] = bld(rw, lane * 16, hbase + (k * 4 + m) * 1024);
    f32x4 c2[4][5] = {};
#pragma unroll
    for (int k = 0; k < 12; ++k) {
      SB();
#pragma unroll
      for (int m = 0; m < 4; ++m)
#pragma unroll
        for (int n = 0; n < 5; ++n) c2[m][n] = MF16B(hf[k][m], wf[k][n], c2[m][n]);
      SB();
      if (k + 4 < 12) {
#pragma unroll
        for (int n = 0; n < 5; ++n) wf[k + 4][n] = bld(rw, lane * 16, wbase + ((k + 4) * 5 + n) * 1024);
      }
      if (k + 4 < 12) {
#pragma unroll
        for (int m = 0; m < 4; ++m) hf[k + 4][m] = bld(rw, lane * 16, hbase + ((k + 4) * 4 + m) * 1024);
      }
    }
    STAMP(6);
    lds_wait(L_SYNC + SY_GD, 4);
    {
      const u32 c16 = lane & 15, q = lane >> 4;
      const u32 pb = w * PSZ + 4 * q * PROW + (4 * (c16 & 3) + (c16 >> 2)) * 4;
#pragma unroll
      for (int m = 0; m < 4; ++m)
#pragma unroll
        for (int n = 0; n < 5; ++n)
#pragma unroll
          for (int r = 0; r < 4; ++r) ldsw<float>(pb + (16 * m + r) * PROW + 64 * n, c2[m][n][r]);
    }
    STAMP(7);
  } else {
    const int aw = wave - 4;
    const int e = ii & 1;
    const u32 sbase = 80 * xcd + 5 * (ii >> 1);
    u32 tag;
    float invs = 0.f;
    if (aw == 3) {
      const int tb = 64 * (xcd & 3) + 32 * (xcd >> 2) + ii;
      const u32 ep = __builtin_amdgcn_raw_buffer_load_b32(rw, 0, WS_EPOCH, 16);
      SB();
      u32 xr[8][5];
#pragma unroll
      for (int r = 0; r < 8; ++r)
#pragma unroll
        for (int c = 0; c < 5; ++c) xr[r][c] = *(const u32*)((const char*)a.x + (8 * tb + r) * 1280 + 256 * c + 4 * lane);
      const int ksg = b % 48, dq = b / 48;
      u32x2 wr[2][8];
      if (b < 240) {
        const int c = lane & 15, kg = lane >> 4;
#pragma unroll
        for (int su = 0; su < 2; ++su)
#pragma unroll
          for (int jj = 0; jj < 8; ++jj) {
            int f = 32 * ksg + 16 * (kg >> 1) + 8 * (jj >> 2) + 4 * (kg & 1) + (jj & 3);
            wr[su][jj] = *(const u32x2*)((const char*)a.w2 + f * 1280 + (128 * dq + 64 * su + 4 * c) * 2);
          }
      }
      SB();
      tag = rfl(ep) + 1;
      if (lane < 9) ldsw<u32>(L_SYNC + 4 * lane, lane == 8 ? tag : 0u);
      WAITL0();
      __builtin_amdgcn_s_barrier();
      SB();
      STAMP(1);
      float S[8], Q[8];
#pragma unroll
      for (int r = 0; r < 8; ++r) {
        float s = 0.f, q = 0.f;
#pragma unroll
        for (int c = 0; c < 5; ++c) {
          b16x2 v = __builtin_bit_cast(b16x2, xr[r][c]);
          s = __builtin_amdgcn_fdot2_f32_bf16(v, __builtin_bit_cast(b16x2, 0x3f803f80u), s, false);
          q = __builtin_amdgcn_fdot2_f32_bf16(v, v, q, false);
        }
        S[r] = s; Q[r] = q;
      }
      auto red32 = [](float x, float y) {
        auto t = __builtin_amdgcn_permlane32_swap(__float_as_uint(x), __float_as_uint(y), false, false);
        return __uint_as_float(t[0]) + __uint_as_float(t[1]);
      };
      auto red16 = [](float x, float y) {
        auto t = __builtin_amdgcn_permlane16_swap(__float_as_uint(x), __float_as_uint(y), false, false);
        float z = __uint_as_float(t[0]) + __uint_as_float(t[1]);
        z += __uint_as_float(__builtin_amdgcn_update_dpp(0, __float_as_uint(z), 0x128, 0xf, 0xf, false));
        z += __uint_as_float(__builtin_amdgcn_update_dpp(0, __float_as_uint(z), 0x124, 0xf, 0xf, false));
        z += __uint_as_float(__builtin_amdgcn_update_dpp(0, __float_as_uint(z), 0x122, 0xf, 0xf, false));
        z += __uint_as_float(__builtin_amdgcn_update_dpp(0, __float_as_uint(z), 0x121, 0xf, 0xf, false));
        return z;
      };
      float rs[2], nb[2];
      u32 gr[2];
#pragma unroll
      for (int h = 0; h < 2; ++h) {
        float zs = red16(red32(S[2 * h], S[2 * h + 4]), red32(S[2 * h + 1], S[2 * h + 5]));
        float zq = red16(red32(Q[2 * h], Q[2 * h + 4]), red32(Q[2 * h + 1], Q[2 * h + 5]));
        float mean = zs * (1.f / 640.f);
        float var = __builtin_fmaxf(__builtin_fmaf(zq, 1.f / 640.f, -(mean * mean)), 0.f);
        float m = rbf(mean);
        float sd = rbf(__builtin_amdgcn_sqrtf(rbf(rbf(var) + 1e-5f)));
        gr[h] = (__float_as_uint(m) >> 16) | (__float_as_uint(sd) & 0xffff0000u);
        rs[h] = __builtin_amdgcn_rcpf(sd);
        nb[h] = -m * rs[h];
      }
      if ((lane & 15) < 2) {
        const int h = lane & 1, rho = lane >> 4;
        u32x2 g = {h ? gr[1] : gr[0], tag};
        __builtin_amdgcn_raw_buffer_store_b64(g, rw, (8 * tb + (rho & 1) + 4 * (rho >> 1) + 2 * h) * 8, WS_STATS, 16);
      }
      STAMP(2);
      if (b < 240) {
        const int c = lane & 15, kg = lane >> 4;
#pragma unroll
        for (int su = 0; su < 2; ++su) {
          const int ntg = 8 * dq + 4 * su + (c >> 2);
          const u32 vo = (((ntg / 5) * 48 + ksg) * 5 + ntg % 5) * 1024 + (kg * 16 + (c & 3)) * 16;
#pragma unroll
          for (int i = 0; i < 4; ++i) {
            u32x4 o;
#pragma unroll
            for (int q = 0; q < 4; ++q) o[q] = __builtin_amdgcn_perm(wr[su][2 * q + 1][i >> 1], wr[su][2 * q][i >> 1], (i & 1) ? 0x07060302u : 0x05040100u);
            bst(o, rw, vo + 64 * i, WS_W2R);
          }
        }
      }
      float rr[8], nn[8];
#pragma unroll
      for (int r = 0; r < 8; ++r) {
        const int h = (r >> 1) & 1, rho = (r & 1) + 2 * (r >> 2);
        rr[r] = __uint_as_float(__builtin_amdgcn_readlane(__float_as_uint(rs[h]), 16 * rho));
        nn[r] = __uint_as_float(__builtin_amdgcn_readlane(__float_as_uint(nb[h]), 16 * rho));
      }
#pragma unroll
      for (int c = 0; c < 5; ++c)
#pragma unroll
        for (int hf = 0; hf < 2; ++hf) {
          float v[8];
#pragma unroll
          for (int r = 0; r < 8; ++r) v[r] = __builtin_fmaf(hf ? bf_hi(xr[r][c]) : bf_lo(xr[r][c]), rr[r], nn[r]);
          u32x4 o = {pk_h(v[0], v[1]), pk_h(v[2], v[3]), pk_h(v[4], v[5]), pk_h(v[6], v[7])};
          bst(o, rw, (tb * 640 + 128 * c + 64 * hf + lane) * 16, WS_UT);
        }
      WAITV(0);
      if (lane == 0) __builtin_amdgcn_raw_buffer_store_b32(tag, rw, b * 4, WS_F0, 16);
      STAMP(3);
      poll256(rw, WS_F0, tag, lane);
      if (lane == 0) {
        __hip_atomic_store((LDSP(u32))(size_t)(L_SYNC + SY_OK0), 1u, __ATOMIC_RELAXED, __HIP_MEMORY_SCOPE_WORKGROUP);
        if (b == 0) __builtin_amdgcn_raw_buffer_store_b32(tag, rw, 0, WS_EPOCH, 16);
      }
      STAMP(4);
    } else {
      const u32 slot = aw == 2 ? sbase + 2 : sbase + 3 * e + aw;
      const u32 d = slot2d(slot);
      const u32 reg = AREG + aw * CHSZ;
      const u16* pp[6] = {a.are, a.aim, a.bre, a.bim, a.cre, a.cim};
      u32 pa[6], pb[6];
#pragma unroll
      for (int i = 0; i < 6; ++i) {
        pa[i] = *(const u32*)(pp[i] + d * 192 + 2 * lane);
        pb[i] = pp[i][d * 192 + 128 + lane];
      }
      const u32 ldt = a.ldt[d];
      SB();
      __builtin_amdgcn_s_barrier();
      FENCE();
      tag = rfl(lds_ld(L_SYNC + SY_TAG));
      STAMP(1);
      const float dt = rbf(__builtin_amdgcn_exp2f(bf_lo(ldt) * 1.44269504089f));
      cf W[3];
      float lr[3], rv[3];
      float mx = 0.f;
#pragma unroll
      for (int it = 0; it < 3; ++it) {
        float v[6];
#pragma unroll
        for (int i = 0; i < 6; ++i) v[i] = it == 0 ? bf_lo(pa[i]) : it == 1 ? bf_hi(pa[i]) : bf_lo(pb[i]);
        lr[it] = (dt * v[0]) * 1.44269504089f;
        rv[it] = (dt * v[1]) * 0.15915494309f;
        W[it] = cmul(cf{v[4], v[5]}, cf{dt * v[2], dt * v[3]});
        mx = __builtin_fmaxf(mx, __builtin_fmaxf(__builtin_fabsf(W[it].x), __builtin_fabsf(W[it].y)));
      }
      {
        u32 m = __float_as_uint(mx);
        m = max(m, (u32)__builtin_amdgcn_update_dpp(0, m, 0x128, 0xf, 0xf, false));
        m = max(m, (u32)__builtin_amdgcn_update_dpp(0, m, 0x124, 0xf, 0xf, false));
        m = max(m, (u32)__builtin_amdgcn_update_dpp(0, m, 0x122, 0xf, 0xf, false));
        m = max(m, (u32)__builtin_amdgcn_update_dpp(0, m, 0x121, 0xf, 0xf, false));
        u32 m0 = __builtin_amdgcn_readlane(m, 0), m1 = __builtin_amdgcn_readlane(m, 16);
        u32 m2 = __builtin_amdgcn_readlane(m, 32), m3 = __builtin_amdgcn_readlane(m, 48);
        u32 eb = max(max(m0, m1), max(m2, m3)) >> 23;
        eb = min(max(eb, 8u), 250u);
        const float sc = __uint_as_float((257u - eb) << 23);
        invs = __uint_as_float((eb - 3u) << 23);
#pragma unroll
        for (int it = 0; it < 3; ++it) { W[it].x *= sc; W[it].y *= sc; }
      }
      f32x16 K = {};
      const u32 wrow = reg + 4 * lane, wcol = reg + SCR_COL + 4 * lane;
      const u32 ra = reg + (lane & 31) * SCR_ROW + (lane >> 5) * 16;
      ldsw<u32>(wcol, 0x00003c00u);
#pragma unroll
      for (int it = 0; it < 3; ++it) {
        cf z1 = cexp_l(lr[it], rv[it], 1.f), z64 = cexp_l(lr[it], rv[it], 64.f);
        cf z2 = csq(z1), z3 = cmul(z2, z1), z4 = csq(z2), z5 = cmul(z4, z1), z6 = csq(z3), z7 = cmul(z4, z3), z8 = csq(z4);
        cf z16 = csq(z8), z24 = cmul(z16, z8), z32 = csq(z16), z40 = cmul(z32, z8), z48 = csq(z24), z56 = cmul(z32, z24);
        cf z128 = csq(z64), z192 = cmul(z128, z64), z256 = csq(z128);
        u32 lo[8] = {0, pk_h(z8.x, -z8.y), pk_h(z16.x, -z16.y), pk_h(z24.x, -z24.y), pk_h(z32.x, -z32.y),
                     pk_h(z40.x, -z40.y), pk_h(z48.x, -z48.y), pk_h(z56.x, -z56.y)};
        u32 hi[4] = {0, pk_h(z64.x, -z64.y), pk_h(z128.x, -z128.y), pk_h(z192.x, -z192.y)};
#pragma unroll
        for (int q = 1; q < 8; ++q) ldsw<u32>(wcol + q * SCR_ROW, lo[q]);
#pragma unroll
        for (int h = 1; h < 4; ++h) {
          ldsw<u32>(wcol + 8 * h * SCR_ROW, hi[h]);
#pragma unroll
          for (int q = 1; q < 8; ++q) ldsw<u32>(wcol + (8 * h + q) * SCR_ROW, cmul16(hi[h], lo[q]));
        }
        u32 zl[9] = {0, pk_h(z1.x, z1.y), pk_h(z2.x, z2.y), pk_h(z3.x, z3.y), pk_h(z4.x, z4.y), pk_h(z5.x, z5.y),
                     pk_h(z6.x, z6.y), pk_h(z7.x, z7.y), pk_h(z8.x, z8.y)};
        cf wc = W[it];
        u32 wb[9];
        wb[0] = pk_h(wc.x, wc.y);
#pragma unroll
        for (int q = 1; q < 9; ++q) { wc = cmul(wc, z256); wb[q] = pk_h(wc.x, wc.y); }
        ldsw<u32>(wrow, wb[0]);
#pragma unroll
        for (int q = 1; q < 8; ++q) ldsw<u32>(wrow + q * SCR_ROW, cmul16(wb[0], zl[q]));
        ldsw<u32>(wrow + 8 * SCR_ROW, wb[1]);
        ldsw<u32>(wrow + 9 * SCR_ROW, cmul16(wb[1], zl[2]));
        ldsw<u32>(wrow + 10 * SCR_ROW, cmul16(wb[1], zl[4]));
        ldsw<u32>(wrow + 11 * SCR_ROW, cmul16(wb[1], zl[6]));
        ldsw<u32>(wrow + 12 * SCR_ROW, wb[2]);
        ldsw<u32>(wrow + 13 * SCR_ROW, cmul16(wb[2], zl[4]));
        ldsw<u32>(wrow + 14 * SCR_ROW, cmul16(wb[2], zl[8]));
        ldsw<u32>(wrow + 15 * SCR_ROW, wb[4]);
        ldsw<u32>(wrow + 16 * SCR_ROW, cmul16(wb[1], zl[8]));
        ldsw<u32>(wrow + 17 * SCR_ROW, wb[5]);
        ldsw<u32>(wrow + 18 * SCR_ROW, wb[6]);
        ldsw<u32>(wrow + 19 * SCR_ROW, wb[7]);
        ldsw<u32>(wrow + 20 * SCR_ROW, wb[3]);
        ldsw<u32>(wrow + 21 * SCR_ROW, cmul16(wb[3], zl[4]));
        ldsw<u32>(wrow + 22 * SCR_ROW, cmul16(wb[3], zl[8]));
        ldsw<u32>(wrow + 23 * SCR_ROW, cmul16(wb[4], zl[8]));
        ldsw<u32>(wrow + 24 * SCR_ROW, wb[8]);
        FENCE();
#pragma unroll
        for (int k = 0; k < 8; ++k) {
          u32x4 fa = ldsr<u32x4>(ra + 32 * k), fb = ldsr<u32x4>(ra + SCR_COL + 32 * k);
          K = MF32H(fa, fb, K);
        }
        FENCE();
      }
      STAMP(2);
      {
        const u32 bq = lane & 31;
        const bool g = lane >= 32;
        ldsw<u32x2>(reg + (16 + 8 * bq + (g ? 4 : 0)) * 2, u32x2{pk_h(K[0], K[1]), pk_h(K[2], K[3])});
        u32x4 o;
        o[0] = pk_h(K[4], K[4]);
        o[1] = pk_h(g ? K[4] : K[5], g ? K[5] : K[6]);
        o[2] = pk_h(g ? K[5] : K[6], g ? K[5] : K[6]);
        o[3] = pk_h(g ? K[6] : K[7], g ? K[6] : K[8]);
        ldsw<u32x4>(reg + (16 + (g ? 512 : 256) + 8 * bq) * 2, o);
        if (g) {
          ldsw<u32x4>(reg + (16 + 768 + 8 * bq) * 2, u32x4{pk_h(K[8], K[8]), pk_h(K[8], K[9]), pk_h(K[9], K[9]), pk_h(K[10], K[10])});
          u32 w0 = pk_h(K[7], K[7]);
          ldsw<u32x4>(reg + (16 + 1024 + 8 * bq) * 2, u32x4{w0, w0, pk_h((bq & 1) ? K[11] : K[7], K[11]), pk_h(K[11], K[11])});
        } else if (bq < 2) {
          ldsw<u32x4>(reg + 16 * bq, u32x4{0, 0, 0, 0});
        }
        u32 t0 = pk_h(g ? K[7] : K[9], K[10]);
        u32 t1 = pk_h(K[11], K[12]);
        const u32 pm = reg + P3O + (bq + (g ? 0 : 32)) * 4;
        ldsw<u16>(pm, (u16)t0);
        ldsw<u16>(pm - 2, (u16)t0);
        if (!g) {
          ldsw<u16>(pm + 128, (u16)(t0 >> 16));
          ldsw<u16>(pm + 126, (u16)(t0 >> 16));
          ldsw<u16>(pm + 256, (u16)t1);
          ldsw<u16>(pm + 254, (u16)t1);
          if (bq == 0) ldsw<u16>(reg + P3O + 510, (u16)(t1 >> 16));
        }
        FENCE();
#pragma unroll
        for (int rnd = 0; rnd < 3; ++rnd) {
          const u32 m = 64 * rnd + lane;
          if (rnd < 2 || m < 162) {
            u32x2 dl = ldsr<u32x2>(reg + 16 * m - 8);
            u32x4 dh = ldsr<u32x4>(reg + 16 * m);
            u32 am = __builtin_amdgcn_alignbit(dl[1], dl[0], 16), a0 = __builtin_amdgcn_alignbit(dh[0], dl[1], 16);
            u32 a1 = __builtin_amdgcn_alignbit(dh[1], dh[0], 16), a2 = __builtin_amdgcn_alignbit(dh[2], dh[1], 16);
            u32 a3 = __builtin_amdgcn_alignbit(dh[3], dh[2], 16);
            ldsw<u32x4>(reg + TABS + 16 * m, u32x4{a0, a1, a2, a3});
            ldsw<u32x4>(reg + 2 * TABS + 16 * m, u32x4{dl[1], dh[0], dh[1], dh[2]});
            ldsw<u32x4>(reg + 3 * TABS + 16 * m, u32x4{am, a0, a1, a2});
          }
        }
        ldsw<u32x4>(reg + UPO + 16 * lane, u32x4{0, 0, 0, 0});
        if (lane == 0) ldsw<float>(L_SYNC + 64 + 4 * aw, invs);
      }
      STAMP(3);
      lds_wait(L_SYNC + SY_OK0, 1);
      {
        const u32 ub = reg + UPO + (aw == 2 ? 1024 : 512);
        u32x4 uu[4];
#pragma unroll
        for (int i = 0; i < 4; ++i) uu[i] = bld(rw, ((64 * i + lane) * 640 + slot) * 16, WS_UT);
#pragma unroll
        for (int i = 0; i < 4; ++i) ldsw<u32x4>(ub + (64 * i + lane) * 16, uu[i]);
      }
      STAMP(4);
    }
    xi0 = *(const u32x4*)((const char*)a.x + (64 * rt + it0 / 10) * 1280 + ct * 160 + (it0 % 10) * 16);
    xi1 = xi0;
    lds_arrive(L_SYNC + SY_A, lane);
    lds_wait(L_SYNC + SY_A, 4);
    STAMP(5);
    {
      const u32 r = lane & 15, g4 = lane >> 4;
      u32 selp[4];
      int lo7;
      {
        const int lam = 16 + (int)r - 8 * (int)g4;
        auto rne8 = [](int x) { return (x + 3 + ((x >> 3) & 1)) >> 3; };
        lo7 = rne8(lam - 7);
#pragma unroll
        for (int i = 0; i < 4; ++i)
          selp[i] = (rne8(lam - 2 * i) > lo7 ? 0x0302u : 0x0100u) | (rne8(lam - 2 * i - 1) > lo7 ? 0x03020000u : 0x01000000u);
      }
      const u32 fl = ((lane >> 2) & 3) * TABS + (32 - 8 * g4 + 4 * (lane & 3) - 4) * 2;
      const u32 pl = P3O + (lo7 - 128) * 4;
      const u32 gl = (16 * r - 16 + 8 * g4) * 2;
      u32x4 F[8], G[8];
      auto ldF = [&](u32 reg, u32 dp) {
#pragma unroll
        for (int m = 0; m < 5; ++m) {
          const u32 ad = reg + fl + 32 * dp + 512 * m;
          s16x4 x0 = ldstr(ad + 8), x1 = ldstr(ad);
          F[m] = __builtin_bit_cast(u32x4, __builtin_shufflevector(x0, x1, 0, 1, 2, 3, 4, 5, 6, 7));
        }
#pragma unroll
        for (int m = 5; m < 8; ++m) {
          u32 pv = ldsr<u32>(reg + pl + 8 * dp + 128 * m);
#pragma unroll
          for (int i = 0; i < 4; ++i) F[m][i] = __builtin_amdgcn_perm(pv, pv, selp[i]);
        }
      };
      f32x4 y1[8] = {}, y2[4] = {};
      {
        const u32 reg = AREG + (aw >> 1) * CHSZ;
        const u32 ub = reg + UPO + 512 + gl;
#pragma unroll
        for (int i = 0; i < 4; ++i) {
          const u32 dp = 8 * (aw & 1) + 2 * i;
          ldF(reg, dp);
#pragma unroll
          for (int q = 0; q < 8; ++q) G[q] = ldsr<u32x4>(ub - 32 * dp + 512 * q);
#pragma unroll
          for (int m = 0; m < 8; ++m)
#pragma unroll
            for (int T = m; T < 8; ++T) y1[T] = MF16H(F[m], G[T - m], y1[T]);
        }
      }
      {
        const u32 reg = AREG + 2 * CHSZ;
        const u32 ub = reg + UPO + 1024 + gl - 512 * e;
#pragma unroll
        for (int i = 0; i < 2; ++i) {
          const u32 dp = 4 * aw + 2 * i;
          ldF(reg, dp);
#pragma unroll
          for (int q = 0; q < 8; ++q) G[q] = ldsr<u32x4>(ub - 32 * dp + 512 * q);
#pragma unroll
          for (int m = 0; m < 8; ++m)
#pragma unroll
            for (int t = m >> 1; t < 4; ++t) y2[t] = MF16H(F[m], G[2 * t + 1 - m], y2[t]);
        }
      }
      STAMP(6);
      lds_wait(L_SYNC + SY_H, 4);
      const u32 hi = aw & 1;
      const u32 mine = L_RED + aw * 7168 + lane * 16;
#pragma unroll
      for (int i = 0; i < 4; ++i) ldsw<f32x4>(mine + 1024 * i, selv4(hi, y1[i], y1[4 + i]));
#pragma unroll
      for (int i = 1; i < 4; ++i) {
        const int t = (aw + i) & 3;
        f32x4 v = selv4(t & 2, selv4(t & 1, y2[3], y2[2]), selv4(t & 1, y2[1], y2[0]));
        ldsw<f32x4>(mine + 1024 * (3 + i), v);
      }
      lds_arrive(L_SYNC + SY_A2, lane);
      lds_wait(L_SYNC + SY_A2, 4);
      f32x4 yo[5];
#pragma unroll
      for (int i = 0; i < 4; ++i) yo[i] = selv4(hi, y1[4 + i], y1[i]) + ldsr<f32x4>(L_RED + (aw ^ 1) * 7168 + lane * 16 + 1024 * i);
      {
        f32x4 own = selv4(aw & 2, selv4(aw & 1, y2[3], y2[2]), selv4(aw & 1, y2[1], y2[0]));
        f32x4 pz[4];
#pragma unroll
        for (int s = 0; s < 4; ++s) {
          const u32 i = (aw - s) & 3;
          f32x4 v = ldsr<f32x4>(L_RED + s * 7168 + lane * 16 + 1024 * (3 + (i ? i : 1)));
          pz[s] = i ? v : own;
        }
        yo[4] = (pz[0] + pz[1]) + (pz[2] + pz[3]);
      }
      const u32 sl1 = sbase + 3 * e + (aw >> 1), sl2 = sbase + 2;
      const float is1 = ldsr<float>(L_SYNC + 64 + 4 * (aw >> 1)), is2 = ldsr<float>(L_SYNC + 64 + 8);
      const float dk1 = bf_lo(a.dsk[slot2d(sl1)]), dk2 = bf_lo(a.dsk[slot2d(sl2)]);
      const u32 T2 = 2 * aw + 1 - e;
      const u32 img = L_SIMG + aw * 4096;
#pragma unroll
      for (int i = 0; i < 5; ++i) {
        const u32 to = (16 * r + 4 * g4) * 2;
        const u32 ua = i < 4 ? AREG + (aw >> 1) * CHSZ + UPO + 512 + 512 * (4 * hi + i) : AREG + 2 * CHSZ + UPO + 1024 + 512 * T2;
        u32x2 uv = ldsr<u32x2>(ua + to);
        const float is = i < 4 ? is1 : is2, dk = i < 4 ? dk1 : dk2;
        float sv[4];
#pragma unroll
        for (int q = 0; q < 4; ++q) {
          const u32 uq = uv[q >> 1];
          h16x2 hh = __builtin_bit_cast(h16x2, uq);
          sv[q] = rbf(yo[i][q] * is) + rbf((float)hh[q & 1] * dk);
        }
        ldsw<u32x2>(img + 512 * i + to, u32x2{pk_bf(sv[0], sv[1]), pk_bf(sv[2], sv[3])});
      }
      FENCE();
      bst(ldsr<u32x4>(img + 16 * lane), rw, sl1 * 4096 + hi * 2048 + 16 * lane, WS_ST);
      bst(ldsr<u32x4>(img + 1024 + 16 * lane), rw, sl1 * 4096 + hi * 2048 + 1024 + 16 * lane, WS_ST);
      if (lane < 32) bst(ldsr<u32x4>(img + 2048 + 16 * lane), rw, sl2 * 4096 + T2 * 512 + 16 * lane, WS_ST);
      WAITV(0);
      WAITL0();
      u32 old = 0;
      if (lane == 0) old = lds_add(L_SYNC + SY_S);
      old = rfl(old);
      if (old == 3 && lane == 0) __builtin_amdgcn_raw_buffer_store_b32(tag, rw, b * 4, WS_F2, 16);
      lds_wait(L_SYNC + SY_S, 4);
      lds_arrive(L_SYNC + SY_GD, lane);
    }
    STAMP(7);
    if (aw == 0) {
      ldsw<float>(L_B2 + 4 * lane, bf_lo(a.b2[80 * ct + lane]));
      if (lane < 16) ldsw<float>(L_B2 + 256 + 4 * lane, bf_lo(a.b2[80 * ct + 64 + lane]));
    }
    poll256(rw, WS_F2, tag, lane);
    STAMP(8);
    {
      const int at = tid - 256;
      u32x4 sv[3];
#pragma unroll
      for (int i = 0; i < 3; ++i) {
        const int id = min(at + 256 * i, 639), dl = id >> 3, tg = id & 7;
        const u32 d = 80 * ct + dl;
        const u32 sl = (d & ~127u) + 64 * (d & 1) + ((d & 127) >> 1);
        sv[i] = bld(rw, sl * 4096 + (64 * rt + 8 * tg) * 2, WS_ST);
      }
#pragma unroll
      for (int i = 0; i < 3; ++i) {
        const int id = min(at + 256 * i, 639), dl = id >> 3, tg = id & 7;
        const u32 ad = L_S2 + 8 * tg * S2ROW + dl * 2;
#pragma unroll
        for (int q = 0; q < 4; ++q) {
          ldsw<u16>(ad + 2 * q * S2ROW, (u16)sv[i][q]);
          ldsw<u16>(ad + (2 * q + 1) * S2ROW, (u16)(sv[i][q] >> 16));
        }
      }
    }
    STAMP(9);
  }
  WAITL0();
  __builtin_amdgcn_s_barrier();
  FENCE();
  STAMP(10);
#pragma unroll
  for (int rnd = 0; rnd < 2; ++rnd) {
    if (rnd == 1 && wave >= 2) break;
    const int id = rnd ? it1 : it0;
    const u32 t = id / 10, dg = id % 10;
    const u32x4 xv = rnd ? xi1 : xi0;
    const u32 pa = t * PROW + dg * 32;
    f32x4 v0 = (ldsr<f32x4>(pa) + ldsr<f32x4>(pa + PSZ)) + (ldsr<f32x4>(pa + 2 * PSZ) + ldsr<f32x4>(pa + 3 * PSZ));
    f32x4 v1 = (ldsr<f32x4>(pa + 16) + ldsr<f32x4>(pa + PSZ + 16)) + (ldsr<f32x4>(pa + 2 * PSZ + 16) + ldsr<f32x4>(pa + 3 * PSZ + 16));
    f32x4 b0 = ldsr<f32x4>(L_B2 + dg * 32), b1 = ldsr<f32x4>(L_B2 + dg * 32 + 16);
    u32x4 sv = ldsr<u32x4>(L_S2 + t * S2ROW + dg * 16);
    float o[8];
#pragma unroll
    for (int q = 0; q < 8; ++q) {
      float pre = rbf(rbf(q < 4 ? v0[q & 3] : v1[q & 3]) + (q < 4 ? b0[q & 3] : b1[q & 3]));
      float g = rbf(pre * __builtin_amdgcn_rcpf(1.f + __builtin_amdgcn_exp2f(pre * -1.44269504089f)));
      float s = (q & 1) ? bf_hi(sv[q >> 1]) : bf_lo(sv[q >> 1]);
      float xx = (q & 1) ? bf_hi(xv[q >> 1]) : bf_lo(xv[q >> 1]);
      o[q] = xx + rbf(s * g);
    }
    u32x4 ov = {pk_bf(o[0], o[1]), pk_bf(o[2], o[3]), pk_bf(o[4], o[5]), pk_bf(o[6], o[7])};
    *(u32x4*)((char*)a.out + (64 * rt + t) * 1280 + ct * 160 + dg * 16) = ov;
  }
  STAMP(11);
}

extern "C" void kernel_launch(void* const* d_in, const int* in_sizes, int n_in, void* d_out, int out_size, void* d_ws,
                              size_t ws_size, hipStream_t stream) {
  Args a;
  a.x = (const u16*)d_in[0]; a.are = (const u16*)d_in[1]; a.aim = (const u16*)d_in[2]; a.bre = (const u16*)d_in[3];
  a.bim = (const u16*)d_in[4]; a.cre = (const u16*)d_in[5]; a.cim = (const u16*)d_in[6]; a.ldt = (const u16*)d_in[7];
  a.dsk = (const u16*)d_in[8]; a.w1 = (const u16*)d_in[10]; a.b1 = (const u16*)d_in[11]; a.w2 = (const u16*)d_in[12];
  a.b2 = (const u16*)d_in[13]; a.out = (u16*)d_out; a.ws = (char*)d_ws;
  (void)hipFuncSetAttribute((const void*)s4_block, hipFuncAttributeMaxDynamicSharedMemorySize, LDS_BYTES);
  hipLaunchKernelGGL(s4_block, dim3(256), dim3(512), LDS_BYTES, stream, a);
}
```

```cpp
#include <hip/hip_runtime.h>
#include <stdint.h>

typedef unsigned short u16;
typedef unsigned int u32;
typedef unsigned long long u64;
typedef short s16x4 __attribute__((ext_vector_type(4)));
typedef short s16x8 __attribute__((ext_vector_type(8)));
typedef _Float16 h16x8 __attribute__((ext_vector_type(8)));
typedef _Float16 h16x2 __attribute__((ext_vector_type(2)));
typedef __bf16 b16x2 __attribute__((ext_vector_type(2)));
typedef __bf16 b16x8 __attribute__((ext_vector_type(8)));
typedef float f32x2 __attribute__((ext_vector_type(2)));
typedef float f32x4 __attribute__((ext_vector_type(4)));
typedef float f32x16 __attribute__((ext_vector_type(16)));
typedef u32 u32x4 __attribute__((ext_vector_type(4)));
typedef u32 u32x2 __attribute__((ext_vector_type(2)));
#define LDSP(T) __attribute__((address_space(3))) T*
#if defined(__HIP_DEVICE_COMPILE__)
#define NOPK __attribute__((target("no-packed-fp32-ops")))
#else
#define NOPK
#endif
#define LAM __attribute__((always_inline)) NOPK
#define DEV __device__ __forceinline__ NOPK

constexpr u32 WS_EPOCH = 0;
constexpr u32 FSTR = 128;
constexpr u32 WS_STATS = 4096;
constexpr u32 WS_F0 = 32768, WS_F1 = 65536, WS_F2 = 98304;
constexpr u32 WS_UT = 131072;
constexpr u32 WS_W2R = WS_UT + 256 * 640 * 16;
constexpr u32 WS_HF = WS_W2R + 8 * 48 * 5 * 1024;
constexpr u32 WS_ST = WS_HF + 32 * 48 * 4 * 1024;
constexpr u32 WS_END = WS_ST + 640 * 2048 * 2;
constexpr u32 WS_DBG = WS_END;

constexpr u32 CHSZ = 16384;
constexpr u32 GB = 3 * CHSZ;
constexpr u32 WSL = 12288, XB = GB + 5 * WSL, XSL = 16384;
#define UNIT(w, i) (GB + 5120u * (w) + ((i) == 3 ? 0u : (i) == 4 ? ((w) < 3 ? 20480u : 62464u) : (i) == 0 ? 82944u : (i) == 1 ? ((w) < 2 ? 35840u : 93184u) : 46080u))
constexpr u32 TABS = 2624;
constexpr u32 P3O = 4 * TABS;
constexpr u32 UPO = P3O + 512;
constexpr u32 SCR_ROW = 272, SCR_COL = 25 * SCR_ROW;
constexpr u32 L_CS = 15616;
constexpr u32 L_SYNC = CHSZ + 15616;
constexpr u32 SY_G = 0, SY_K = 4, SY_A = 8, SY_CS = 12, SY_H = 16, SY_S = 20, SY_A2 = 24, SY_T = 28, SY_TOK = 32, SY_W = 36, SY_I = 40, SY_U = 44, SY_TAG = 48;
constexpr u32 L_S2 = 0;
constexpr u32 L_X2 = CHSZ;
constexpr u32 LDS_BYTES = 163840;
#define ROT1 (((ii >> 2) + 3 * (ii & 3)) % 10)
#define ROT2 (((3 * ct) / 2 + 3 * (ii & 3)) % 12)
#define GD 0
#define F2SL 8
#define F2N 2
#define F2K 5
#define UGATE 0
#define GPRIO 0
#define W7U 3
#define W7P 3
#define CDM 2
#define KP 1
#define SYI 0
#define XP 1
#define BDM 0
#define ADM 0
#define UW 4
#define OAUX 17
#define OW 1
#define TW 0
#define KD 0
#define SPIN 1
#define XU 1
#define WFE 0
#define DDM 0
#define EDM 0
#define PRIO_ITEMS 1
#define PRIO(n) __builtin_amdgcn_s_setprio(n)
constexpr u32 UMASK = 0xBFF8BFF8u;
#define UPOLL_SLEEP 8
constexpr int SPIN_MAX = 1 << 19;
#define s2d(s) (10 * ((s) & 63) + ((s) >> 6))

struct Args {
  const u16 *x, *are, *aim, *bre, *bim, *cre, *cim, *ldt, *dsk, *w1, *b1, *w2, *b2;
  u16* out;
  char* ws;
};

DEV u32 f2u(float v) { return __builtin_bit_cast(u32, v); }
DEV float u2f(u32 v) { return __builtin_bit_cast(float, v); }
DEV u32 umax(u32 x, u32 y) { return x > y ? x : y; }
DEV u32 umin(u32 x, u32 y) { return x < y ? x : y; }
DEV float bf_lo(u32 v) { return u2f(v << 16); }
DEV float bf_hi(u32 v) { return u2f(v & 0xffff0000u); }
DEV u32 pk_bf(float a, float b) { return __builtin_bit_cast(u32, __builtin_convertvector((f32x2){a, b}, b16x2)); }
DEV u32 pk_h(float a, float b) { return __builtin_bit_cast(u32, __builtin_convertvector((f32x2){a, b}, h16x2)); }
DEV float rbf(float a) { return bf_lo(pk_bf(a, a)); }
DEV u32 rfl(u32 v) { return __builtin_amdgcn_readfirstlane(v); }
template <class T> DEV T ldsr(u32 a) { return *(LDSP(T))(size_t)(a); }
template <class T> DEV void ldsw(u32 a, T v) { *(LDSP(T))(size_t)(a) = v; }
DEV s16x4 ldstr(u32 a) { return __builtin_amdgcn_ds_read_tr16_b64_v4i16((LDSP(s16x4))(size_t)(a)); }
DEV u32 lds_ld(u32 a) { return __hip_atomic_load((LDSP(u32))(size_t)(a), __ATOMIC_RELAXED, __HIP_MEMORY_SCOPE_WORKGROUP); }
DEV u32 lds_add(u32 a) { return __hip_atomic_fetch_add((LDSP(u32))(size_t)(a), 1u, __ATOMIC_RELAXED, __HIP_MEMORY_SCOPE_WORKGROUP); }
#define FENCE() asm volatile("" ::: "memory")
#define WAITV(n) asm volatile("s_waitcnt vmcnt(" #n ")" ::: "memory")
#define WAITL0() asm volatile("s_waitcnt lgkmcnt(0)" ::: "memory")
DEV void lds_arrive(u32 a) {
  asm volatile("s_mov_b64 exec, 1\n\tds_add_u32 %0, %1\n\ts_mov_b64 exec, -1" ::"v"(a), "v"(1u) : "memory");
}
#define WAITVN(n) asm volatile("s_waitcnt vmcnt(%0)" ::"n"(n) : "memory")
DEV void lds_wait(u32 a, u32 n) {
  FENCE();
#pragma unroll 1
  for (int it = 0; it < SPIN_MAX; ++it) {
    if (rfl(lds_ld(a)) >= n) break;
    __builtin_amdgcn_s_sleep(1);
  }
  FENCE();
}
DEV void lds_spin(u32 a, u32 n) {
  FENCE();
#pragma unroll 1
  for (int it = 0; it < SPIN_MAX; ++it) {
    if (rfl(lds_ld(a)) >= n) break;
    if (!SPIN) __builtin_amdgcn_s_sleep(1);
  }
  FENCE();
}
DEV void setm0(u32 v) { asm volatile("s_mov_b32 m0, %0\n\ts_nop 0" ::"s"(v) : "memory"); }
#define DMA(voff, sbase, off) asm volatile("global_load_lds_dwordx4 %0, %1 offset:%2" ::"v"(voff), "s"(sbase), "n"(off) : "memory")
#define DMAC(voff, sbase, off) asm volatile("global_load_lds_dwordx4 %0, %1 offset:%2 sc1" ::"v"(voff), "s"(sbase), "n"(off) : "memory")
#define TIDW(v, off) asm volatile("ds_write_addtid_b32 %0 offset:%1" ::"v"(v), "n"(off) : "memory")
typedef u32 u32x4a __attribute__((ext_vector_type(4), aligned(4)));
DEV u32 cmul16(u32 a, u32 b) {
  u32 t, r;
  asm("v_pk_mul_f16 %0, %1, %2 op_sel:[1,1] op_sel_hi:[1,0] neg_lo:[1,0]" : "=v"(t) : "v"(a), "v"(b));
  asm("v_pk_fma_f16 %0, %1, %2, %3 op_sel:[0,0,0] op_sel_hi:[0,1,1]" : "=v"(r) : "v"(a), "v"(b), "v"(t));
  return r;
}
DEV f32x4 selv4(bool c, f32x4 x, f32x4 y) { asm("" : "+v"(x)); asm("" : "+v"(y)); return c ? x : y; }
DEV f32x4 tr4(f32x4 v) {
  auto a = __builtin_amdgcn_permlane32_swap(f2u(v[0]), f2u(v[2]), false, false);
  auto b = __builtin_amdgcn_permlane32_swap(f2u(v[1]), f2u(v[3]), false, false);
  auto c = __builtin_amdgcn_permlane16_swap(a[0], b[0], false, false);
  auto d = __builtin_amdgcn_permlane16_swap(a[1], b[1], false, false);
  return f32x4{u2f(c[0]), u2f(c[1]), u2f(d[0]), u2f(d[1])};
}
struct cf { float x, y; };
DEV cf cmul(cf a, cf b) { return {__builtin_fmaf(a.x, b.x, -(a.y * b.y)), __builtin_fmaf(a.x, b.y, a.y * b.x)}; }
DEV cf csq(cf a) { return {__builtin_fmaf(a.x, a.x, -(a.y * a.y)), (a.x + a.x) * a.y}; }
DEV cf cexp_l(float lr, float rev, float l) {
  float m = __builtin_amdgcn_exp2f(lr * l), p = __builtin_amdgcn_fractf(rev * l);
  return {m * __builtin_amdgcn_cosf(p), m * __builtin_amdgcn_sinf(p)};
}
#define MF32B(a, b, c) __builtin_amdgcn_mfma_f32_32x32x16_bf16(__builtin_bit_cast(b16x8, a), __builtin_bit_cast(b16x8, b), c, 0, 0, 0)
#define MF16B(a, b, c) __builtin_amdgcn_mfma_f32_16x16x32_bf16(__builtin_bit_cast(b16x8, a), __builtin_bit_cast(b16x8, b), c, 0, 0, 0)
#define MF32H(a, b, c) __builtin_amdgcn_mfma_f32_32x32x16_f16(__builtin_bit_cast(h16x8, a), __builtin_bit_cast(h16x8, b), c, 0, 0, 0)
#define MF16H(a, b, c) __builtin_amdgcn_mfma_f32_16x16x32_f16(__builtin_bit_cast(h16x8, a), __builtin_bit_cast(h16x8, b), c, 0, 0, 0)
template <int V> struct IC { static constexpr int v = V; };
template <int I, int N, class F> DEV void sfor(F&& f) { if constexpr (I < N) { f(IC<I>{}); sfor<I + 1, N>(f); } }
#define SB() __builtin_amdgcn_sched_barrier(0)

#define STAMP(i) do {} while (0)
#define STAMP_INIT() do {} while (0)
#define STAMP_FLUSH() do {} while (0)

typedef __amdgpu_buffer_rsrc_t rsrc_t;
DEV u32x4 bld(rsrc_t r, u32 v, u32 s) { return __builtin_amdgcn_raw_buffer_load_b128(r, v, s, 16); }
DEV void bst(u32x4 d, rsrc_t r, u32 v, u32 s) { __builtin_amdgcn_raw_buffer_store_b128(d, r, v + s, 0, 16); }

constexpr int G1W[9] = {7, 11, 14, 14, 18, 18, 18, 25, 18};
constexpr int G1X[9] = {63, 10, 10, 7, 7, 7, 7, 14, 10};
constexpr int g2n(int j, int s) {
  int pos = 0, at = -1;
  for (int k = 0; k < 4; ++k)
    for (int m = 0; m < 4; ++m) { ++pos; if (k == j && m == s) at = pos; }
  for (int k = 0; k <= j; ++k)
    for (int m = 0; m < 4; ++m) {
      if (k == j && m == s) return pos - at;
      if (m == 0 && k >= 2 && k <= 5) pos += 5;
      if (k + 4 <= 11) { ++pos; if (k + 4 == j && m == s) at = pos; }
    }
  return 0;
}
struct G2T {
  int v[9][4];
  constexpr G2T() : v{} { for (int j = 0; j < 9; ++j) for (int s = 0; s < 4; ++s) v[j][s] = g2n(j, s); }
};
constexpr G2T G2N;
#define ASM_COL \
  "v_pk_mul_f16 %0, %6, %9 op_sel:[1,1] op_sel_hi:[1,0] neg_lo:[1,0]\n" \
  "ds_write_addtid_b32 %9 offset:7072\n" \
  "v_pk_mul_f16 %1, %6, %10 op_sel:[1,1] op_sel_hi:[1,0] neg_lo:[1,0]\n" \
  "v_pk_fma_f16 %0, %6, %9, %0 op_sel:[0,0,0] op_sel_hi:[0,1,1]\n" \
  "v_pk_mul_f16 %2, %6, %11 op_sel:[1,1] op_sel_hi:[1,0] neg_lo:[1,0]\n" \
  "ds_write_addtid_b32 %0 offset:9248\n" \
  "v_pk_fma_f16 %1, %6, %10, %1 op_sel:[0,0,0] op_sel_hi:[0,1,1]\n" \
  "ds_write_addtid_b32 %10 offset:7344\n" \
  "v_pk_mul_f16 %3, %6, %12 op_sel:[1,1] op_sel_hi:[1,0] neg_lo:[1,0]\n" \
  "ds_write_addtid_b32 %1 offset:9520\n" \
  "v_pk_fma_f16 %2, %6, %11, %2 op_sel:[0,0,0] op_sel_hi:[0,1,1]\n" \
  "v_pk_mul_f16 %4, %6, %13 op_sel:[1,1] op_sel_hi:[1,0] neg_lo:[1,0]\n" \
  "ds_write_addtid_b32 %2 offset:9792\n" \
  "v_pk_fma_f16 %3, %6, %12, %3 op_sel:[0,0,0] op_sel_hi:[0,1,1]\n" \
  "ds_write_addtid_b32 %11 offset:7616\n" \
  "v_pk_mul_f16 %5, %6, %14 op_sel:[1,1] op_sel_hi:[1,0] neg_lo:[1,0]\n" \
  "ds_write_addtid_b32 %3 offset:10064\n" \
  "v_pk_fma_f16 %4, %6, %13, %4 op_sel:[0,0,0] op_sel_hi:[0,1,1]\n" \
  "v_pk_mul_f16 %0, %6, %15 op_sel:[1,1] op_sel_hi:[1,0] neg_lo:[1,0]\n" \
  "ds_write_addtid_b32 %4 offset:10336\n" \
  "v_pk_fma_f16 %5, %6, %14, %5 op_sel:[0,0,0] op_sel_hi:[0,1,1]\n" \
  "ds_write_addtid_b32 %12 offset:7888\n" \
  "v_pk_mul_f16 %1, %7, %9 op_sel:[1,1] op_sel_hi:[1,0] neg_lo:[1,0]\n" \
  "ds_write_addtid_b32 %5 offset:10608\n" \
  "v_pk_fma_f16 %0, %6, %15, %0 op_sel:[0,0,0] op_sel_hi:[0,1,1]\n" \
  "v_pk_mul_f16 %2, %7, %10 op_sel:[1,1] op_sel_hi:[1,0] neg_lo:[1,0]\n" \
  "ds_write_addtid_b32 %0 offset:10880\n" \
  "v_pk_fma_f16 %1, %7, %9, %1 op_sel:[0,0,0] op_sel_hi:[0,1,1]\n" \
  "ds_write_addtid_b32 %13 offset:8160\n" \
  "v_pk_mul_f16 %3, %7, %11 op_sel:[1,1] op_sel_hi:[1,0] neg_lo:[1,0]\n" \
  "ds_write_addtid_b32 %1 offset:11424\n" \
  "v_pk_fma_f16 %2, %7, %10, %2 op_sel:[0,0,0] op_sel_hi:[0,1,1]\n" \
  "v_pk_mul_f16 %4, %7, %12 op_sel:[1,1] op_sel_hi:[1,0] neg_lo:[1,0]\n" \
  "ds_write_addtid_b32 %2 offset:11696\n" \
  "v_pk_fma_f16 %3, %7, %11, %3 op_sel:[0,0,0] op_sel_hi:[0,1,1]\n" \
  "ds_write_addtid_b32 %14 offset:8432\n" \
  "v_pk_mul_f16 %5, %7, %13 op_sel:[1,1] op_sel_hi:[1,0] neg_lo:[1,0]\n" \
  "ds_write_addtid_b32 %3 offset:11968\n" \
  "v_pk_fma_f16 %4, %7, %12, %4 op_sel:[0,0,0] op_sel_hi:[0,1,1]\n" \
  "v_pk_mul_f16 %0, %7, %14 op_sel:[1,1] op_sel_hi:[1,0] neg_lo:[1,0]\n" \
  "ds_write_addtid_b32 %4 offset:12240\n" \
  "v_pk_fma_f16 %5, %7, %13, %5 op_sel:[0,0,0] op_sel_hi:[0,1,1]\n" \
  "ds_write_addtid_b32 %15 offset:8704\n" \
  "v_pk_mul_f16 %1, %7, %15 op_sel:[1,1] op_sel_hi:[1,0] neg_lo:[1,0]\n" \
  "ds_write_addtid_b32 %5 offset:12512\n" \
  "v_pk_fma_f16 %0, %7, %14, %0 op_sel:[0,0,0] op_sel_hi:[0,1,1]\n" \
  "v_pk_mul_f16 %2, %8, %9 op_sel:[1,1] op_sel_hi:[1,0] neg_lo:[1,0]\n" \
  "ds_write_addtid_b32 %0 offset:12784\n" \
  "v_pk_fma_f16 %1, %7, %15, %1 op_sel:[0,0,0] op_sel_hi:[0,1,1]\n" \
  "ds_write_addtid_b32 %6 offset:8976\n" \
  "v_pk_mul_f16 %3, %8, %10 op_sel:[1,1] op_sel_hi:[1,0] neg_lo:[1,0]\n" \
  "ds_write_addtid_b32 %1 offset:13056\n" \
  "v_pk_fma_f16 %2, %8, %9, %2 op_sel:[0,0,0] op_sel_hi:[0,1,1]\n" \
  "v_pk_mul_f16 %4, %8, %11 op_sel:[1,1] op_sel_hi:[1,0] neg_lo:[1,0]\n" \
  "ds_write_addtid_b32 %2 offset:13600\n" \
  "v_pk_fma_f16 %3, %8, %10, %3 op_sel:[0,0,0] op_sel_hi:[0,1,1]\n" \
  "ds_write_addtid_b32 %7 offset:11152\n" \
  "v_pk_mul_f16 %5, %8, %12 op_sel:[1,1] op_sel_hi:[1,0] neg_lo:[1,0]\n" \
  "ds_write_addtid_b32 %3 offset:13872\n" \
  "v_pk_fma_f16 %4, %8, %11, %4 op_sel:[0,0,0] op_sel_hi:[0,1,1]\n" \
  "v_pk_mul_f16 %0, %8, %13 op_sel:[1,1] op_sel_hi:[1,0] neg_lo:[1,0]\n" \
  "ds_write_addtid_b32 %4 offset:14144\n" \
  "v_pk_fma_f16 %5, %8, %12, %5 op_sel:[0,0,0] op_sel_hi:[0,1,1]\n" \
  "ds_write_addtid_b32 %8 offset:13328\n" \
  "v_pk_mul_f16 %1, %8, %14 op_sel:[1,1] op_sel_hi:[1,0] neg_lo:[1,0]\n" \
  "ds_write_addtid_b32 %5 offset:14416\n" \
  "v_pk_fma_f16 %0, %8, %13, %0 op_sel:[0,0,0] op_sel_hi:[0,1,1]\n" \
  "v_pk_mul_f16 %2, %8, %15 op_sel:[1,1] op_sel_hi:[1,0] neg_lo:[1,0]\n" \
  "ds_write_addtid_b32 %0 offset:14688\n" \
  "v_pk_fma_f16 %1, %8, %14, %1 op_sel:[0,0,0] op_sel_hi:[0,1,1]\n" \
  "ds_write_addtid_b32 %1 offset:14960\n" \
  "v_pk_fma_f16 %2, %8, %15, %2 op_sel:[0,0,0] op_sel_hi:[0,1,1]\n" \
  "ds_write_addtid_b32 %2 offset:15232\n"
#define ASM_ROW \
  "v_pk_mul_f16 %0, %6, %15 op_sel:[1,1] op_sel_hi:[1,0] neg_lo:[1,0]\n" \
  "ds_write_addtid_b32 %6 offset:0\n" \
  "v_pk_mul_f16 %1, %6, %16 op_sel:[1,1] op_sel_hi:[1,0] neg_lo:[1,0]\n" \
  "v_pk_fma_f16 %0, %6, %15, %0 op_sel:[0,0,0] op_sel_hi:[0,1,1]\n" \
  "v_pk_mul_f16 %2, %6, %17 op_sel:[1,1] op_sel_hi:[1,0] neg_lo:[1,0]\n" \
  "ds_write_addtid_b32 %0 offset:272\n" \
  "v_pk_fma_f16 %1, %6, %16, %1 op_sel:[0,0,0] op_sel_hi:[0,1,1]\n" \
  "ds_write_addtid_b32 %7 offset:2176\n" \
  "v_pk_mul_f16 %3, %6, %18 op_sel:[1,1] op_sel_hi:[1,0] neg_lo:[1,0]\n" \
  "ds_write_addtid_b32 %1 offset:544\n" \
  "v_pk_fma_f16 %2, %6, %17, %2 op_sel:[0,0,0] op_sel_hi:[0,1,1]\n" \
  "v_pk_mul_f16 %4, %6, %19 op_sel:[1,1] op_sel_hi:[1,0] neg_lo:[1,0]\n" \
  "ds_write_addtid_b32 %2 offset:816\n" \
  "v_pk_fma_f16 %3, %6, %18, %3 op_sel:[0,0,0] op_sel_hi:[0,1,1]\n" \
  "ds_write_addtid_b32 %8 offset:3264\n" \
  "v_pk_mul_f16 %5, %6, %20 op_sel:[1,1] op_sel_hi:[1,0] neg_lo:[1,0]\n" \
  "ds_write_addtid_b32 %3 offset:1088\n" \
  "v_pk_fma_f16 %4, %6, %19, %4 op_sel:[0,0,0] op_sel_hi:[0,1,1]\n" \
  "v_pk_mul_f16 %0, %6, %21 op_sel:[1,1] op_sel_hi:[1,0] neg_lo:[1,0]\n" \
  "ds_write_addtid_b32 %4 offset:1360\n" \
  "v_pk_fma_f16 %5, %6, %20, %5 op_sel:[0,0,0] op_sel_hi:[0,1,1]\n" \
  "ds_write_addtid_b32 %9 offset:5440\n" \
  "v_pk_mul_f16 %1, %7, %16 op_sel:[1,1] op_sel_hi:[1,0] neg_lo:[1,0]\n" \
  "ds_write_addtid_b32 %5 offset:1632\n" \
  "v_pk_fma_f16 %0, %6, %21, %0 op_sel:[0,0,0] op_sel_hi:[0,1,1]\n" \
  "v_pk_mul_f16 %2, %7, %18 op_sel:[1,1] op_sel_hi:[1,0] neg_lo:[1,0]\n" \
  "ds_write_addtid_b32 %0 offset:1904\n" \
  "v_pk_fma_f16 %1, %7, %16, %1 op_sel:[0,0,0] op_sel_hi:[0,1,1]\n" \
  "ds_write_addtid_b32 %10 offset:4080\n" \
  "v_pk_mul_f16 %3, %7, %20 op_sel:[1,1] op_sel_hi:[1,0] neg_lo:[1,0]\n" \
  "ds_write_addtid_b32 %1 offset:2448\n" \
  "v_pk_fma_f16 %2, %7, %18, %2 op_sel:[0,0,0] op_sel_hi:[0,1,1]\n" \
  "v_pk_mul_f16 %4, %7, %22 op_sel:[1,1] op_sel_hi:[1,0] neg_lo:[1,0]\n" \
  "ds_write_addtid_b32 %2 offset:2720\n" \
  "v_pk_fma_f16 %3, %7, %20, %3 op_sel:[0,0,0] op_sel_hi:[0,1,1]\n" \
  "ds_write_addtid_b32 %11 offset:4624\n" \
  "v_pk_mul_f16 %5, %8, %18 op_sel:[1,1] op_sel_hi:[1,0] neg_lo:[1,0]\n" \
  "ds_write_addtid_b32 %3 offset:2992\n" \
  "v_pk_fma_f16 %4, %7, %22, %4 op_sel:[0,0,0] op_sel_hi:[0,1,1]\n" \
  "v_pk_mul_f16 %0, %8, %22 op_sel:[1,1] op_sel_hi:[1,0] neg_lo:[1,0]\n" \
  "ds_write_addtid_b32 %4 offset:4352\n" \
  "v_pk_fma_f16 %5, %8, %18, %5 op_sel:[0,0,0] op_sel_hi:[0,1,1]\n" \
  "ds_write_addtid_b32 %12 offset:4896\n" \
  "v_pk_mul_f16 %1, %9, %18 op_sel:[1,1] op_sel_hi:[1,0] neg_lo:[1,0]\n" \
  "ds_write_addtid_b32 %5 offset:3536\n" \
  "v_pk_fma_f16 %0, %8, %22, %0 op_sel:[0,0,0] op_sel_hi:[0,1,1]\n" \
  "v_pk_mul_f16 %2, %9, %22 op_sel:[1,1] op_sel_hi:[1,0] neg_lo:[1,0]\n" \
  "ds_write_addtid_b32 %0 offset:3808\n" \
  "v_pk_fma_f16 %1, %9, %18, %1 op_sel:[0,0,0] op_sel_hi:[0,1,1]\n" \
  "ds_write_addtid_b32 %13 offset:5168\n" \
  "v_pk_mul_f16 %3, %10, %22 op_sel:[1,1] op_sel_hi:[1,0] neg_lo:[1,0]\n" \
  "ds_write_addtid_b32 %1 offset:5712\n" \
  "v_pk_fma_f16 %2, %9, %22, %2 op_sel:[0,0,0] op_sel_hi:[0,1,1]\n" \
  "ds_write_addtid_b32 %2 offset:5984\n" \
  "v_pk_fma_f16 %3, %10, %22, %3 op_sel:[0,0,0] op_sel_hi:[0,1,1]\n" \
  "ds_write_addtid_b32 %14 offset:6528\n" \
  "ds_write_addtid_b32 %3 offset:6256\n"

__global__ __launch_bounds__(512, 2) NOPK void s4_block(Args a) {
  extern __shared__ __attribute__((aligned(16))) char smem[];
  const int tid = __builtin_amdgcn_workitem_id_x(), lane = tid & 63;
  u32 pf;
  {
    u64 pc;
    u32 t0, t1, t2;
    asm volatile(
        "v_readfirstlane_b32 %2, %5\n\t"
        "s_getpc_b64 %1\n"
        "s4_pc:\n\t"
        "s_lshr_b32 %4, %6, 3\n\t"
        "s_mulk_i32 %4, 1408\n\t"
        "s_addk_i32 %4, -12288\n\t"
        "s_mov_b32 %3, s4_k-s4_pc-8192\n\t"
        "s_cmpk_lt_u32 %2, 256\n\t"
        "s_cselect_b32 %3, s4_g-s4_pc, %3\n\t"
        "s_cmpk_lt_u32 %2, 384\n\t"
        "s_cselect_b32 %3, %3, %4\n\t"
        "s_cmpk_lt_u32 %2, 448\n\t"
        "s_cselect_b32 %3, %3, s4_l-s4_pc-14336\n\t"
        "s_lshl_b32 %2, %2, 5\n\t"
        "s_add_u32 %3, %3, %2\n\t"
        "v_and_b32 %0, 15, %5\n\t"
        "v_lshl_add_u32 %0, %0, 7, %3\n\t"
        "v_min_u32 %0, s4_end-s4_pc, %0\n\t"
        "global_load_dword %0, %0, %1"
        : "=&v"(pf), "=&s"(pc), "=&s"(t0), "=&s"(t1), "=&s"(t2) : "v"(tid), "s"(__builtin_amdgcn_workgroup_id_x()) : "scc");
  }
  if (tid >= 448 && tid < 460) ldsw<u32>(L_SYNC + 4 * lane, 0u);
  WAITL0();
  __builtin_amdgcn_s_barrier();
  FENCE();
  const int wave = rfl(tid >> 6);
  const int b = __builtin_amdgcn_workgroup_id_x(), xcd = b & 7, ii = b >> 3;
  const rsrc_t rw = __builtin_amdgcn_make_buffer_rsrc((void*)a.ws, 0, 0x7fffffff, 0x00020000);
  const int rt = 8 * (xcd & 3) + 4 * (xcd >> 2) + (ii & 3), ct = ii >> 2;
  STAMP_INIT();
  const u32 q4 = lane >> 4, c16 = lane & 15;
  f32x4 own[3];
  u32x2 xv[3];
  f32x4 xf[3];
  auto x_unpack = [&]() LAM {
#pragma unroll
    for (int n = 0; n < 3; ++n) {
      xf[n] = f32x4{bf_lo(xv[n][0]), bf_hi(xv[n][0]), bf_lo(xv[n][1]), bf_hi(xv[n][1])};
      if (XU) asm volatile("" : "+v"(xf[n]));
    }
  };
  u32 eo, es, ep1, ep2, ep3, ep0;
  auto epi_setup = [&]() LAM {
    const u32 m = wave & 3, n0 = wave < 4 ? 0 : 3;
    eo = (64 * rt + 16 * m + c16) * 1280 + 160 * ct + 32 * n0 + 8 * q4;
#pragma unroll
    for (int n = 0; n < 3; ++n) xv[n] = *(const u32x2*)((const char*)a.x + eo + (wave < 4 || n < 2 ? 32 * n : 0));
    es = L_S2 + (16 * n0 + 4 * q4 + ((lane >> 2) & 3)) * 128 + (16 * m + 4 * (lane & 3)) * 2;
    ep0 = UNIT(m, 4) + n0 * 1024 + lane * 16;
    ep1 = UNIT((m + 1) & 3, 0) + n0 * 1024 + lane * 16;
    ep2 = UNIT((m + 2) & 3, 2) + n0 * 1024 + lane * 16;
    ep3 = UNIT((m + 3) & 3, 3) + n0 * 1024 + lane * 16;
    asm volatile("" : "+v"(eo), "+v"(es), "+v"(ep0), "+v"(ep1), "+v"(ep2), "+v"(ep3));
  };

  if (wave < 4) {
    asm volatile("s4_g:");
    const int w = wave;
    const int p = 4 * (xcd & 3) + (ii & 3), j = 8 * (xcd >> 2) + (ii >> 2);
    const int wt = w == 3 ? 0 : w;
    u32 voffW[3], voffX[4];
#pragma unroll
    for (int u = 0; u < 3; ++u) {
      u32 off = 1024 * (3 * w + u) + 16 * lane;
      u32 row = off / 192;
      voffW[u] = row * 3072 + (off - row * 192) - 1024 * u;
    }
#pragma unroll
    for (int q = 0; q < 4; ++q) voffX[q] = (8 * q + (lane >> 3)) * 1280 + (((lane & 7) ^ ((4 * q + (lane >> 4)) & 7)) * 16) - 1024 * q;
    const char* gW = (const char*)a.w1 + j * 192;
    const char* gX = (const char*)a.x + (128 * p + 32 * w) * 1280;
    const int rot1 = ROT1;
    auto dmx = [&](int c, int q) LAM {
      if (q == 0) setm0(XB + (c % 3) * XSL + 4096 * w);
      const char* g = gX + 128 * (c + rot1 >= 10 ? c + rot1 - 10 : c + rot1);
      if (q == 0) DMA(voffX[0], g, 0); else if (q == 1) DMA(voffX[1], g, 1024); else if (q == 2) DMA(voffX[2], g, 2048); else DMA(voffX[3], g, 3072);
    };
    auto dmw = [&](int c, int u) LAM {
      if (u == 0) setm0(GB + (c % 5) * WSL + 3072 * w);
      const char* g = gW + (c + rot1 >= 10 ? c + rot1 - 10 : c + rot1) * 64 * 3072;
      if (u == 0) DMA(voffW[0], g, 0); else if (u == 1) DMA(voffW[1], g, 1024); else DMA(voffW[2], g, 2048);
    };
    auto allx = [&](int c) LAM { dmx(c, 0); dmx(c, 1); dmx(c, 2); dmx(c, 3); };
    auto allw = [&](int c) LAM { dmw(c, 0); dmw(c, 1); dmw(c, 2); };
    SB();
    allx(0); allw(0);
    if (KD) lds_arrive(L_SYNC + SY_H);
    u32 b1v[3];
    u32x2 b2v[5];
    if (SYI == 0) lds_wait(L_SYNC + SY_I, 4);
    allx(1); allw(1);
    SB();

    u32 ab[3], bb[4];
    {
      const u32 q = (lane >> 2) & 3, pp = lane & 3;
      const u32 base = GB + (8 * (q4 >> 1) + q) * 192 + (16 * (q4 & 1) + 4 * pp) * 2;
#pragma unroll
      for (int i = 0; i < 3; ++i) ab[i] = base + 64 * ((wt + i) % 3);
      const u32 t = lane & 31, g = lane >> 5;
#pragma unroll
      for (int k = 0; k < 4; ++k) bb[k] = XB + 4096 * w + t * 128 + (((2 * k + g) ^ ((t >> 1) & 7)) * 16);
    }
    const u32 selv = ((lane == 0 || lane == 32 || lane == 20 || lane == 52) ? 0x3f803f80u : 0u);
    const u32x4 sel = {selv, selv, selv, selv};
    f32x16 acc[3] = {};
    f32x4 csacc = {};
    s16x8 fa[3][3];
    u32x4 fb[3];
    auto rdA = [&](int s, int i) LAM {
      if (s > 39) return;
      const u32 so = ((s >> 2) % 5) * WSL + 16 * (s & 3) * 192;
      s16x4 lo = ldstr(ab[i] + so), hi = ldstr(ab[i] + so + 4 * 192);
      fa[s % 3][i] = __builtin_shufflevector(lo, hi, 0, 1, 2, 3, 4, 5, 6, 7);
    };
    auto rdB = [&](int s) LAM {
      if (s > 39) return;
      fb[s % 3] = ldsr<u32x4>(bb[s & 3] + ((s >> 2) % 3) * XSL);
    };
    WAITV(7);
    asm volatile("" ::"v"(pf));
    lds_arrive(L_SYNC + SY_G);
    lds_spin(L_SYNC + SY_G, 4);
    STAMP(12);
    rdB(0); rdA(0, 0); rdA(0, 1); rdA(0, 2);
    rdB(1); rdA(1, 0); rdA(1, 1); rdA(1, 2);
    SB();
    csacc = MF16B(sel, fa[0][0], csacc);
    u32 cv = 0, tag = 0;
    u32x2 gran = {0, 0};
    u32 fw2 = 0;
    const u32 fo2 = (((80 * ct + ((lane & 16) ? 79 : 0)) >> 7) * 48 + 12 * w + umin(c16, 11)) * FSTR;
    sfor<0, 40>([&](auto S) LAM {
      constexpr int s = decltype(S)::v, c = s >> 2, k = s & 3;
      SB();
      if ((k == 0 && c > 0 && c < 9) || s == 2) WAITVN(G1W[c < 9 ? c : 0]);
      if ((k == 0 && c > 0) || s == 2) lds_arrive(L_SYNC + SY_G);
      SB();
      acc[0] = MF32B(fa[s % 3][0], fb[s % 3], acc[0]);
      SB();
      if (k == 2 && c < 9) {
        if (rfl(cv) < 4 * (c + 2)) lds_spin(L_SYNC + SY_G, 4 * (c + 2));
        if (c >= 1) WAITVN(G1X[c < 9 ? c : 0]);
        FENCE();
        if (c == 3) STAMP(8);
        if (c == 6) STAMP(9);
      }
      if (k == 3 && c <= 6) dmx(c + 3, 0);
      if (s == 1) dmx(2, 0);
      rdB(s + 2); rdA(s + 2, 0);
      SB();
      acc[1] = MF32B(fa[s % 3][1], fb[s % 3], acc[1]);
      SB();
      if (s == 0 || s == 2 || s == 5) dmw(s == 0 ? 2 : s == 2 ? 3 : 4, 0);
      if (k == 2 && c >= 1 && c <= 5) dmw(c + 4, 0);
      if (k == 3 && c <= 6) dmx(c + 3, 1);
      if (s == 1) dmx(2, 1);
      rdA(s + 2, 1);
      if (s == 39) {
        float cs = csacc[0];
        u32 hi = pk_bf(cs, cs) & 0xffffu;
        u32 lo = pk_bf(cs - bf_lo(hi), 0.f);
        if (w < 3 && lane < 32) ldsw<u32>(L_CS + 128 * w + 4 * lane, hi | (lo << 16));
        FENCE();
        lds_arrive(L_SYNC + SY_CS);
      }
      SB();
      acc[2] = MF32B(fa[s % 3][2], fb[s % 3], acc[2]);
      SB();
      if (s == 0 || s == 2 || s == 5) dmw(s == 0 ? 2 : s == 2 ? 3 : 4, 1);
      if (k == 2 && c >= 1 && c <= 5) dmw(c + 4, 1);
      if (k == 3 && c <= 6) dmx(c + 3, 2);
      if (s == 1) dmx(2, 2);
      rdA(s + 2, 2);
      if (k == 1 && c > 0 && c < 9) cv = lds_ld(L_SYNC + SY_G);
      SB();
      if (s < 39) csacc = MF16B(sel, fa[(s + 1) % 3][0], csacc);
      SB();
      if (s == 0 || s == 2 || s == 5) dmw(s == 0 ? 2 : s == 2 ? 3 : 4, 2);
      if (k == 2 && c >= 1 && c <= 5) dmw(c + 4, 2);
      if (k == 3 && c <= 6) dmx(c + 3, 3);
      if (s == 1) dmx(2, 3);
      if (s == 23) { lds_wait(L_SYNC + SY_TOK, 1); tag = rfl(lds_ld(L_SYNC + SY_TAG)); }
      if (s == 27) {
        SB();
        gran = __builtin_amdgcn_raw_buffer_load_b64(rw, (128 * p + 32 * w + (lane & 31)) * 8, WS_STATS, 16);
        fw2 = __builtin_amdgcn_raw_buffer_load_b32(rw, fo2, WS_F0, 16);
#pragma unroll
        for (int i = 0; i < 3; ++i) b1v[i] = a.b1[96 * j + 32 * ((wt + i) % 3) + (lane & 31)];
#pragma unroll
        for (int n = 0; n < 5; ++n) b2v[n] = *(const u32x2*)(a.b2 + 80 * ct + 16 * n + 4 * q4);
      }
    });
    SB();
    STAMP(2);
    if (__builtin_amdgcn_ballot_w64(gran[1] != tag) != 0) {
#pragma unroll 1
      for (int it = 0; it < SPIN_MAX; ++it) {
        gran = __builtin_amdgcn_raw_buffer_load_b64(rw, (128 * p + 32 * w + (lane & 31)) * 8, WS_STATS, 16);
        if (__builtin_amdgcn_ballot_w64(gran[1] != tag) == 0) break;
        __builtin_amdgcn_s_sleep(2);
      }
    }
    lds_spin(L_SYNC + SY_CS, 4);
    {
      const bool g0 = lane < 32;
      u32 nm = (gran[0] & 0xffffu) ^ 0x8000u;
      u32x4 fbx = {g0 ? (nm | (nm << 16)) : 0u, g0 ? (gran[0] >> 16) : 0u, 0u, 0u};
#pragma unroll
      for (int i = 0; i < 3; ++i) {
        u32 cs = ldsr<u32>(L_CS + 128 * ((wt + i) % 3) + 4 * (lane & 31));
        u32x4 fax = {g0 ? cs : 0u, g0 ? b1v[i] : 0u, 0u, 0u};
        acc[i] = MF32B(fax, fbx, acc[i]);
      }
    }
    STAMP(3);
    if (GPRIO) PRIO(GPRIO);
    if (__builtin_amdgcn_ballot_w64(fw2 != tag) != 0) {
#pragma unroll 1
      for (int it = 0; it < SPIN_MAX; ++it) {
        fw2 = __builtin_amdgcn_raw_buffer_load_b32(rw, fo2, WS_F0, 16);
        if (__builtin_amdgcn_ballot_w64(fw2 != tag) == 0) break;
        __builtin_amdgcn_s_sleep(2);
      }
    }
    FENCE();
    const u32 l16 = lane * 16;
    const int rot2 = ROT2;
    const char* g2 = a.ws + WS_W2R + (ct * 48 + 12 * w) * 5120;
    auto dm2 = [&](int ks, int u) LAM {
      if (u == 0) setm0(UNIT(w, ks % 5));
      if (u == 4) setm0(UNIT(w, ks % 5) + 4096);
      const char* g = g2 + (ks + rot2 >= 12 ? ks + rot2 - 12 : ks + rot2) * 5120;
      if (u == 0) DMAC(l16, g, 0); else if (u == 1) DMAC(l16, g, 1024); else if (u == 2) DMAC(l16, g, 2048);
      else if (u == 3) DMAC(l16, g, 3072); else DMAC(l16, g + 4096, 0);
    };
    {
      const float sd = bf_hi(gran[0]), r = __builtin_amdgcn_rcpf(sd);
      const float k0 = -2.3022082f * r, k1 = -0.10294324f * r * r * r;
      const u32 vo = (2 * (w & 1) + ((lane >> 4) & 1)) * 1024 + ((lane & 15) + 16 * (lane >> 5)) * 16;
#pragma unroll
      for (int i = 0; i < 3; ++i) {
        const u32 so = WS_HF + ((2 * p + (w >> 1)) * 48 + 3 * j + (wt + i) % 3) * 4096;
#pragma unroll
        for (int hh = 0; hh < 2; ++hh) {
          float hv[8];
#pragma unroll
          for (int e = 0; e < 8; ++e) {
            if (GD > 0) {
              SB();
              const int z = 16 * i + 8 * hh + e, d0 = z * GD / 46, d1 = (z + 1) * GD / 46;
              if (z < 46 && d1 > d0) dm2(6 + d0 / 5, d0 % 5);
              SB();
            }
            float v = acc[i][8 * hh + e];
            float ex = __builtin_amdgcn_exp2f(__builtin_fmaf(v * v, k1, k0) * v);
            hv[e] = v * __builtin_amdgcn_rcpf(__builtin_fmaf(ex, sd, sd));
          }
          u32x4 o = {pk_bf(hv[0], hv[1]), pk_bf(hv[2], hv[3]), pk_bf(hv[4], hv[5]), pk_bf(hv[6], hv[7])};
          bst(o, rw, vo + hh * 512, so);
        }
      }
    }
    SB();
    STAMP(10);
    u32x4 wf[3][5];
    auto ldW = [&]() LAM {
#pragma unroll
      for (int k = 0; k < 3; ++k)
#pragma unroll
        for (int n = 0; n < 5; ++n)
          asm volatile("buffer_load_dwordx4 %0, %1, %2, %3 offen offset:%4 sc1" : "=v"(wf[k][n]) : "v"(n == 4 ? l16 + 4096 : l16), "s"(rw),
                       "s"(WS_W2R + (ct * 48 + 12 * w + (k + rot2 >= 12 ? k + rot2 - 12 : k + rot2)) * 5120), "n"(n == 4 ? 0 : 1024 * n) : "memory");
    };
    if (WFE) { ldW(); WAITV(15); } else WAITV(0);
    if (lane == 0) __builtin_amdgcn_raw_buffer_store_b32(tag, rw, (p * 16 + j) * FSTR + 4 * w, WS_F1, 16);
    STAMP(4);
    if (!WFE) ldW();
    SB();
#pragma unroll
    for (int u = GD; u < (DDM ? 0 : 10); ++u) dm2(6 + u / 5, u % 5);
    SB();
    epi_setup();
    SB();
    f32x4 c2[4][5] = {};
#pragma unroll
    for (int n = 0; n < 5; ++n) c2[3][n] = tr4(f32x4{bf_lo(b2v[n][0]), bf_hi(b2v[n][0]), bf_lo(b2v[n][1]), bf_hi(b2v[n][1])});
    u32 ub[5];
#pragma unroll
    for (int i = 0; i < 5; ++i) ub[i] = UNIT(w, i) + l16;
    auto rdW = [&](int ks, int n) LAM { wf[ks % 3][n] = ldsr<u32x4>(ub[ks % 5] + 1024 * n); };
    SB();
#pragma unroll 1
    for (int it = 0; it < SPIN_MAX; ++it) {
      u32 f = __builtin_amdgcn_raw_buffer_load_b32(rw, ((rt >> 1) * 16 + 4 * w + (lane & 3)) * FSTR + 8 * (rt & 1) + (lane & 4), WS_F1, 16);
      if (__builtin_amdgcn_ballot_w64(f != tag) == 0) break;
      __builtin_amdgcn_s_sleep(1);
    }
    lds_wait(L_SYNC + SY_U, 4);
#pragma unroll
    for (int k = 0; k < 3; ++k)
      asm volatile("s_waitcnt vmcnt(0)" : "+v"(wf[k][0]), "+v"(wf[k][1]), "+v"(wf[k][2]), "+v"(wf[k][3]), "+v"(wf[k][4])::"memory");
    STAMP(5);
    u32 hv[4];
#pragma unroll
    for (int s = 0; s < 4; ++s) hv[s] = lane * 16 + ((w + 1 + s) & 3) * 1024;
    const u32 hbase = WS_HF + (rt * 48 + 12 * w) * 4096;
    u32x4 hf[4][4];
    auto ldH = [&](int ks, int s) LAM {
      asm volatile("buffer_load_dwordx4 %0, %1, %2, %3 offen sc1" : "=v"(hf[ks & 3][s]) : "v"(hv[s]), "s"(rw), "s"(hbase + (ks + rot2 >= 12 ? ks + rot2 - 12 : ks + rot2) * 4096));
    };
#pragma unroll
    for (int k = 0; k < 4; ++k)
#pragma unroll
      for (int s = 0; s < 4; ++s) ldH(k, s);
#pragma unroll
    for (int k = 0; k < 9; ++k) {
      SB();
      if (k >= 2 && k <= 5) WAITL0();
      if (k == F2K) lds_arrive(L_SYNC + SY_W);
#pragma unroll
      for (int s = 0; s < 4; ++s) {
        asm volatile("s_waitcnt vmcnt(%1)" : "+v"(hf[k & 3][s]) : "n"(G2N.v[k][s]));
#pragma unroll
        for (int n = 0; n < 5; ++n) {
          SB();
          c2[s][n] = MF16B(wf[k % 3][n], hf[k & 3][s], c2[s][n]);
          SB();
          if (s == 0 && k >= 2 && k <= 5) dm2(k + 6, n);
          if (s == 1 && k >= 1) rdW(k + 2, n);
        }
        SB();
        if (k + 4 <= 11) ldH(k + 4, s);
      }
    }
    STAMP(6);
#pragma unroll
    for (int s = 0; s < 4; ++s) {
      SB();
      asm volatile("s_waitcnt vmcnt(%3)" : "+v"(hf[1][s]), "+v"(hf[2][s]), "+v"(hf[3][s]) : "n"(3 - s));
#pragma unroll
      for (int k = 9; k < 12; ++k)
#pragma unroll
        for (int n = 0; n < 5; ++n) {
          SB();
          c2[s][n] = MF16B(wf[k % 3][n], hf[k & 3][s], c2[s][n]);
          SB();
          if (s == 0 && k == 9) rdW(11, n);
          if (s > 0 && k == 10) ldsw<f32x4>(ub[s == 1 ? 3 : s == 2 ? 2 : 0] + n * 1024, c2[s - 1][n]);
        }
    }
    SB();
    x_unpack();
    SB();
    ldsw<f32x4>(ub[4] + 3 * 1024, c2[3][3]);
    ldsw<f32x4>(ub[4] + 4 * 1024, c2[3][4]);
#pragma unroll
    for (int n = 0; n < 3; ++n) own[n] = c2[3][n];
    STAMP(7);
  } else {
    const int aw = wave - 4;
    if (KP || aw == 3) PRIO(3);
    const int e = ii & 1;
    const u32 sbase = 80 * xcd + 5 * (ii >> 1);
    const u32 sl1 = sbase + 3 * e + (aw >> 1), sl2 = sbase + 2;
    const u32 dk1r = a.dsk[s2d(sl1)], dk2r = a.dsk[s2d(sl2)];
    u32 tag;
    if (aw == 3) {
      asm volatile("s4_l:");
      const int tb = 64 * (xcd & 3) + 32 * (xcd >> 2) + ii;
      const u32 ep = __builtin_amdgcn_raw_buffer_load_b32(rw, 0, WS_EPOCH, 16);
      SB();
      u32 xr[8][5];
#pragma unroll
      for (int r = 0; r < 8; ++r) {
        const char* px = (const char*)a.x + (8 * tb + r) * 1280 + 20 * lane;
        u32x4a v = *(const u32x4a*)px;
        xr[r][0] = v[0]; xr[r][1] = v[1]; xr[r][2] = v[2]; xr[r][3] = v[3];
        xr[r][4] = *(const u32*)(px + 16);
      }
      SB();
      SB();
      lds_arrive(L_SYNC + SY_I);
      tag = rfl(ep) + 1;
      asm volatile("" ::"v"(pf), "s"(tag));
      if (lane == 0) {
        ldsw<u32>(L_SYNC + SY_TAG, tag);
        FENCE();
        ldsw<u32>(L_SYNC + SY_TOK, 1u);
      }
      FENCE();
      STAMP(1);
      float S[8], Q[8];
#pragma unroll
      for (int r = 0; r < 8; ++r) {
        float s = 0.f, q = 0.f;
#pragma unroll
        for (int c = 0; c < 5; ++c) {
          b16x2 v = __builtin_bit_cast(b16x2, xr[r][c]);
          s = __builtin_amdgcn_fdot2_f32_bf16(v, __builtin_bit_cast(b16x2, 0x3f803f80u), s, false);
          q = __builtin_amdgcn_fdot2_f32_bf16(v, v, q, false);
        }
        S[r] = s; Q[r] = q;
      }
      SB();
      const int bq = b < 240 ? b : b - 16;
      const int ksg = bq % 48, dq = bq / 48;
      u32x2 wr[2][8];
      {
#pragma unroll
        for (int su = 0; su < 2; ++su)
#pragma unroll
          for (int jj = 0; jj < 8; ++jj) {
            const u32 f = 32 * ksg + 16 * (q4 >> 1) + 8 * (jj >> 2) + 4 * (q4 & 1) + (jj & 3);
            wr[su][jj] = *(const u32x2*)((const char*)a.w2 + f * 1280 + (128 * dq + 64 * su + 4 * c16) * 2);
          }
      }
      SB();
      SB();
      auto red32 = [](float x, float y) LAM {
        auto t = __builtin_amdgcn_permlane32_swap(f2u(x), f2u(y), false, false);
        return u2f(t[0]) + u2f(t[1]);
      };
      auto red16 = [](float x, float y) LAM {
        auto t = __builtin_amdgcn_permlane16_swap(f2u(x), f2u(y), false, false);
        float z = u2f(t[0]) + u2f(t[1]);
        z += u2f(__builtin_amdgcn_update_dpp(0, f2u(z), 0x128, 0xf, 0xf, false));
        z += u2f(__builtin_amdgcn_update_dpp(0, f2u(z), 0x124, 0xf, 0xf, false));
        z += u2f(__builtin_amdgcn_update_dpp(0, f2u(z), 0x122, 0xf, 0xf, false));
        z += u2f(__builtin_amdgcn_update_dpp(0, f2u(z), 0x121, 0xf, 0xf, false));
        return z;
      };
      float rs[2], nb[2];
      u32 gr[2];
#pragma unroll
      for (int h = 0; h < 2; ++h) {
        float zs = red16(red32(S[2 * h], S[2 * h + 4]), red32(S[2 * h + 1], S[2 * h + 5]));
        float zq = red16(red32(Q[2 * h], Q[2 * h + 4]), red32(Q[2 * h + 1], Q[2 * h + 5]));
        float mean = zs * (1.f / 640.f);
        float var = __builtin_fmaxf(__builtin_fmaf(zq, 1.f / 640.f, -(mean * mean)), 0.f);
        float m = rbf(mean);
        float sd = rbf(__builtin_amdgcn_sqrtf(rbf(rbf(var) + 1e-5f)));
        gr[h] = (f2u(m) >> 16) | (f2u(sd) & 0xffff0000u);
        rs[h] = __builtin_amdgcn_rcpf(sd) * 0.0625f;
        nb[h] = -m * rs[h];
      }
      if ((lane & 15) < 2) {
        const int h = lane & 1, rho = lane >> 4;
        u32x2 g = {h ? gr[1] : gr[0], tag};
        __builtin_amdgcn_raw_buffer_store_b64(g, rw, (8 * tb + (rho & 1) + 4 * (rho >> 1) + 2 * h) * 8, WS_STATS, 16);
      }
      float rr[8], nn[8];
#pragma unroll
      for (int r = 0; r < 8; ++r) {
        const int h = (r >> 1) & 1, rho = (r & 1) + 2 * (r >> 2);
        rr[r] = u2f(__builtin_amdgcn_readlane(f2u(rs[h]), 16 * rho));
        nn[r] = u2f(__builtin_amdgcn_readlane(f2u(nb[h]), 16 * rho));
      }
      u32 tb4[4];
#pragma unroll
      for (int i = 0; i < 4; ++i) {
        const u32 y = tag >> (8 * i);
        tb4[i] = (y & 7u) | ((y & 8u) << 11) | ((y & 0x70u) << 12) | ((y & 0x80u) << 23);
      }
#pragma unroll
      for (int c = 0; c < 5; ++c)
#pragma unroll
        for (int hf = 0; hf < 2; ++hf) {
          float v[8];
#pragma unroll
          for (int r = 0; r < 8; ++r) v[r] = __builtin_fmaf(hf ? bf_hi(xr[r][c]) : bf_lo(xr[r][c]), rr[r], nn[r]);
          u32x4 o;
#pragma unroll
          for (int i = 0; i < 4; ++i) o[i] = ((pk_h(v[2 * i], v[2 * i + 1]) + 0x00040004u) & UMASK) | tb4[i];
          bst(o, rw, (tb * 640 + 128 * c + 64 * hf + lane) * 16, WS_UT);
        }
      STAMP(10);
      {
#pragma unroll
        for (int su = 0; su < 2; ++su) {
          const u32 ntg = 8 * dq + 4 * su + (c16 >> 2);
          const u32 vo = (((ntg / 5) * 48 + ksg) * 5 + ntg % 5) * 1024 + (q4 * 16 + (c16 & 3)) * 16;
#pragma unroll
          for (int i = 0; i < 4; ++i) {
            u32x4 o;
#pragma unroll
            for (int q = 0; q < 4; ++q) o[q] = __builtin_amdgcn_perm(wr[su][2 * q + 1][i >> 1], wr[su][2 * q][i >> 1], (i & 1) ? 0x07060302u : 0x05040100u);
            bst(o, rw, vo + 64 * i, WS_W2R);
          }
        }
      }
      WAITV(0);
      if (lane == 0) __builtin_amdgcn_raw_buffer_store_b32(tag, rw, b * FSTR, WS_F0, 16);
      PRIO(W7P);
      u32x4 uu[12];
      if (UGATE) lds_wait(L_SYNC + SY_G, UGATE);
      {
        u32 bad[12];
#pragma unroll
        for (int i = 0; i < 12; ++i) bad[i] = ~0u;
        const u32 l3 = lane / 3, r3 = lane - 3 * l3;
#pragma unroll 1
        for (int it = 0; it < SPIN_MAX; ++it) {
#pragma unroll
          for (int i = 0; i < 12; ++i)
            if (bad[i] != 0) {
              const u32 t3 = r3 + (64 * i) % 3, cy = t3 >= 3;
              uu[i] = bld(rw, ((l3 + (64 * i) / 3 + cy) * 640 + t3 - 3 * cy) * 16, WS_UT + (sbase + 2 * e) * 16);
            }
          u32 any = 0;
#pragma unroll
          for (int i = 0; i < 12; ++i) {
            bad[i] = ((uu[i][0] ^ tb4[0]) | (uu[i][1] ^ tb4[1]) | (uu[i][2] ^ tb4[2]) | (uu[i][3] ^ tb4[3])) & ~UMASK;
            any |= bad[i];
          }
          if (__builtin_amdgcn_ballot_w64(any != 0) == 0) break;
          __builtin_amdgcn_s_sleep(UPOLL_SLEEP);
        }
        FENCE();
      }
      if (b == 0 && lane == 0) __builtin_amdgcn_raw_buffer_store_b32(tag, rw, 0, WS_EPOCH, 16);
      STAMP(2);
      const u32 fo = (((80 * ct + (lane >= 48 ? 79 : 0)) >> 7) * 48 + (lane >= 48 ? lane - 48 : lane)) * FSTR;
      const u32 fp = (((80 * ct + 79) >> 7) * 48 + 16 + (lane & 31)) * FSTR;
      u32 f0 = __builtin_amdgcn_raw_buffer_load_b32(rw, fo, WS_F0, 16), f1 = __builtin_amdgcn_raw_buffer_load_b32(rw, fp, WS_F0, 16);
      SB();
      lds_wait(L_SYNC + SY_K, 3);
#pragma unroll
      for (int ch = 0; ch < 3; ++ch) ldsw<u32x4>(ch * CHSZ + UPO + 16 * lane, u32x4{0, 0, 0, 0});
      {
        const u32 l3 = lane / 3, r3 = lane - 3 * l3;
#pragma unroll
        for (int i = 0; i < 12; ++i) {
          const u32 t3 = r3 + (64 * i) % 3, cy = t3 >= 3, col = t3 - 3 * cy;
          const u32 ch = e ? (col == 0 ? 2 : col - 1) : col;
          ldsw<u32x4>(ch * CHSZ + UPO + (ch == 2 ? 1024 : 512) + (l3 + (64 * i) / 3 + cy) * 16, uu[i] & UMASK);
        }
      }
      SB();
      if (__builtin_amdgcn_ballot_w64(f0 != tag || f1 != tag) != 0) {
#pragma unroll 1
        for (int it = 0; it < SPIN_MAX; ++it) {
          f0 = __builtin_amdgcn_raw_buffer_load_b32(rw, fo, WS_F0, 16);
          f1 = __builtin_amdgcn_raw_buffer_load_b32(rw, fp, WS_F0, 16);
          if (__builtin_amdgcn_ballot_w64(f0 != tag || f1 != tag) == 0) break;
          __builtin_amdgcn_s_sleep(2);
        }
      }
      FENCE();
      STAMP(3);
      {
        const int rot2 = ROT2;
        const u32 l16 = lane * 16;
#pragma unroll
        for (int ks = 3; ks < (ADM ? 3 : BDM ? 4 : CDM ? 5 : 6); ++ks) {
          lds_wait(L_SYNC + SY_G, 4 * (ks + 6));
          const char* g = a.ws + WS_W2R + (ct * 48 + (ks + rot2 >= 12 ? ks + rot2 - 12 : ks + rot2)) * 5120;
#pragma unroll
          for (int w = 0; w < 4; ++w) {
            setm0(UNIT(w, ks % 5));
            DMAC(l16, g + w * 61440, 0); DMAC(l16, g + w * 61440, 1024); DMAC(l16, g + w * 61440, 2048); DMAC(l16, g + w * 61440, 3072);
            setm0(UNIT(w, ks % 5) + 4096);
            DMAC(l16, g + w * 61440 + 4096, 0);
          }
        }
      }
      PRIO(0);
      STAMP(12);
    } else {
      asm volatile("s4_k:");
      const u32 slot = aw == 2 ? sbase + 2 : sbase + 3 * e + aw;
      const u32 d = s2d(slot);
      const u32 reg = aw * CHSZ;
      const u16* pp[6] = {a.are, a.aim, a.bre, a.bim, a.cre, a.cim};
      u32 pa[6], pb[6];
      if (KD) lds_wait(L_SYNC + SY_H, KD);
#pragma unroll
      for (int i = 0; i < 6; ++i) {
        pa[i] = *(const u32*)(pp[i] + d * 192 + 2 * lane);
        pb[i] = pp[i][d * 192 + 128 + lane];
      }
      const u32 ldt = a.ldt[d];
      SB();
      lds_arrive(L_SYNC + SY_I);
      STAMP(1);
      if (PRIO_ITEMS == 0) PRIO(0);
      setm0(reg);
      TIDW(0x00003c00u, SCR_COL);
      const float dt = rbf(__builtin_amdgcn_exp2f(bf_lo(ldt) * 1.44269504089f));
      asm volatile("" ::"v"(pf), "v"(dt));
      cf W[3];
      float lr[3], rv[3];
      float mx = 0.f;
#pragma unroll
      for (int it = 0; it < 3; ++it) {
        float v[6];
#pragma unroll
        for (int i = 0; i < 6; ++i) v[i] = it == 0 ? bf_lo(pa[i]) : it == 1 ? bf_hi(pa[i]) : bf_lo(pb[i]);
        lr[it] = (dt * v[0]) * 1.44269504089f;
        rv[it] = (dt * v[1]) * 0.15915494309f;
        W[it] = cmul(cf{v[4], v[5]}, cf{dt * v[2], dt * v[3]});
        mx = __builtin_fmaxf(mx, __builtin_fmaxf(__builtin_fabsf(W[it].x), __builtin_fabsf(W[it].y)));
      }
      float invs;
      {
        u32 m = f2u(mx);
        m = umax(m, (u32)__builtin_amdgcn_update_dpp(0, m, 0x128, 0xf, 0xf, false));
        m = umax(m, (u32)__builtin_amdgcn_update_dpp(0, m, 0x124, 0xf, 0xf, false));
        m = umax(m, (u32)__builtin_amdgcn_update_dpp(0, m, 0x122, 0xf, 0xf, false));
        m = umax(m, (u32)__builtin_amdgcn_update_dpp(0, m, 0x121, 0xf, 0xf, false));
        u32 m0 = __builtin_amdgcn_readlane(m, 0), m1 = __builtin_amdgcn_readlane(m, 16);
        u32 m2 = __builtin_amdgcn_readlane(m, 32), m3 = __builtin_amdgcn_readlane(m, 48);
        u32 eb = umax(umax(m0, m1), umax(m2, m3)) >> 23;
        eb = umin(umax(eb, 8u), 250u);
        const float sc = u2f((257u - eb) << 23);
        invs = u2f((eb + 1u) << 23);
#pragma unroll
        for (int it = 0; it < 3; ++it) { W[it].x *= sc; W[it].y *= sc; }
      }
      f32x16 K = {};
      const u32 ra = reg + (lane & 31) * SCR_ROW + (lane >> 5) * 16;
      u32x4 ka[8], kb[8];
#define KM(k) do { if (it > 0) { SB(); K = MF32H(ka[k], kb[k], K); SB(); } } while (0)
#pragma unroll
      for (int it = 0; it < 3; ++it) {
        SB();
        cf z1 = cexp_l(lr[it], rv[it], 1.f), c64 = cexp_l(lr[it], rv[it], 64.f);
        c64.y = -c64.y;
        KM(0);
        cf z2 = csq(z1), z3 = cmul(z2, z1), z4 = csq(z2);
        KM(1);
        cf z5 = cmul(z4, z1), z6 = csq(z3), z7 = cmul(z4, z3), z8 = csq(z4);
        KM(2);
        cf c8 = {z8.x, -z8.y};
        cf c16_ = csq(c8), c24 = cmul(c16_, c8), c32 = csq(c16_);
        KM(3);
        cf c40 = cmul(c32, c8), c48 = csq(c24), c56 = cmul(c32, c24);
        KM(4);
        cf c128 = csq(c64), c192 = cmul(c128, c64), z256 = csq(c128);
        z256.y = -z256.y;
        KM(5);
        u32 lo[8] = {0, 0, pk_h(c16_.x, c16_.y), pk_h(c24.x, c24.y), pk_h(c32.x, c32.y), pk_h(c40.x, c40.y), pk_h(c48.x, c48.y), pk_h(c56.x, c56.y)};
        asm("v_cvt_pk_f16_f32 %0, %1, -%2" : "=v"(lo[1]) : "v"(z8.x), "v"(z8.y));
        KM(6);
        u32 hi[4] = {0, pk_h(c64.x, c64.y), pk_h(c128.x, c128.y), pk_h(c192.x, c192.y)};
        u32 zl[9] = {0, pk_h(z1.x, z1.y), pk_h(z2.x, z2.y), pk_h(z3.x, z3.y), pk_h(z4.x, z4.y), pk_h(z5.x, z5.y),
                     pk_h(z6.x, z6.y), pk_h(z7.x, z7.y), pk_h(z8.x, z8.y)};
        KM(7);
        u32 t0, t1, t2, t3, t4, t5;
        asm volatile(ASM_COL : "=&v"(t0), "=&v"(t1), "=&v"(t2), "=&v"(t3), "=&v"(t4), "=&v"(t5)
                     : "v"(hi[1]), "v"(hi[2]), "v"(hi[3]), "v"(lo[1]), "v"(lo[2]), "v"(lo[3]), "v"(lo[4]), "v"(lo[5]), "v"(lo[6]), "v"(lo[7]) : "memory");
        cf wc = W[it];
        u32 wb[9];
        wb[0] = pk_h(wc.x, wc.y);
#pragma unroll
        for (int q = 1; q < 9; ++q) { wc = cmul(wc, z256); wb[q] = pk_h(wc.x, wc.y); }
        asm volatile(ASM_ROW : "=&v"(t0), "=&v"(t1), "=&v"(t2), "=&v"(t3), "=&v"(t4), "=&v"(t5)
                     : "v"(wb[0]), "v"(wb[1]), "v"(wb[2]), "v"(wb[3]), "v"(wb[4]), "v"(wb[5]), "v"(wb[6]), "v"(wb[7]), "v"(wb[8]),
                       "v"(zl[1]), "v"(zl[2]), "v"(zl[3]), "v"(zl[4]), "v"(zl[5]), "v"(zl[6]), "v"(zl[7]), "v"(zl[8]) : "memory");
        FENCE();
        SB();
#pragma unroll
        for (int k = 0; k < 8; ++k) { ka[k] = ldsr<u32x4>(ra + 32 * k); kb[k] = ldsr<u32x4>(ra + SCR_COL + 32 * k); }
        FENCE();
        if (it == 0) STAMP(10); else if (it == 1) STAMP(12);
        if (it + 1 == PRIO_ITEMS) PRIO(0);
      }
      lds_arrive(L_SYNC + SY_K);
      SB();
#pragma unroll
      for (int k = 0; k < 8; ++k) K = MF32H(ka[k], kb[k], K);
      STAMP(2);
      {
        const u32 bq = lane & 31;
        const bool g = lane >= 32;
        ldsw<u32x2>(reg + (16 + 8 * bq + (g ? 4 : 0)) * 2, u32x2{pk_h(K[0], K[1]), pk_h(K[2], K[3])});
        u32x4 o;
        o[0] = pk_h(K[4], K[4]);
        o[1] = pk_h(g ? K[4] : K[5], g ? K[5] : K[6]);
        o[2] = pk_h(g ? K[5] : K[6], g ? K[5] : K[6]);
        o[3] = pk_h(g ? K[6] : K[7], g ? K[6] : K[8]);
        ldsw<u32x4>(reg + (16 + (g ? 512 : 256) + 8 * bq) * 2, o);
        if (g) {
          ldsw<u32x4>(reg + (16 + 768 + 8 * bq) * 2, u32x4{pk_h(K[8], K[8]), pk_h(K[8], K[9]), pk_h(K[9], K[9]), pk_h(K[10], K[10])});
          u32 w0 = pk_h(K[7], K[7]);
          ldsw<u32x4>(reg + (16 + 1024 + 8 * bq) * 2, u32x4{w0, w0, pk_h((bq & 1) ? K[11] : K[7], K[11]), pk_h(K[11], K[11])});
        } else if (bq < 2) {
          ldsw<u32x4>(reg + 16 * bq, u32x4{0, 0, 0, 0});
        }
        u32 t0 = pk_h(g ? K[7] : K[9], K[10]);
        u32 t1 = pk_h(K[11], K[12]);
        const u32 pm = reg + P3O + (bq + (g ? 0 : 32)) * 4;
        ldsw<u16>(pm, (u16)t0);
        ldsw<u16>(pm - 2, (u16)t0);
        if (!g) {
          ldsw<u16>(pm + 128, (u16)(t0 >> 16));
          ldsw<u16>(pm + 126, (u16)(t0 >> 16));
          ldsw<u16>(pm + 256, (u16)t1);
          ldsw<u16>(pm + 254, (u16)t1);
          if (bq == 0) ldsw<u16>(reg + P3O + 510, (u16)(t1 >> 16));
        }
        FENCE();
        u32x2 dl[3];
        u32x4 dh[3];
#pragma unroll
        for (int rnd = 0; rnd < 3; ++rnd) {
          const u32 m = rnd < 2 ? 64 * rnd + lane : umin(128 + lane, 161);
          dl[rnd] = ldsr<u32x2>(reg + 16 * m - 8);
          dh[rnd] = ldsr<u32x4>(reg + 16 * m);
        }
#pragma unroll
        for (int rnd = 0; rnd < 3; ++rnd) {
          const u32 m = rnd < 2 ? 64 * rnd + lane : umin(128 + lane, 161);
          u32 am = __builtin_amdgcn_alignbit(dl[rnd][1], dl[rnd][0], 16), a0 = __builtin_amdgcn_alignbit(dh[rnd][0], dl[rnd][1], 16);
          u32 a1 = __builtin_amdgcn_alignbit(dh[rnd][1], dh[rnd][0], 16), a2 = __builtin_amdgcn_alignbit(dh[rnd][2], dh[rnd][1], 16);
          u32 a3 = __builtin_amdgcn_alignbit(dh[rnd][3], dh[rnd][2], 16);
          ldsw<u32x4>(reg + TABS + 16 * m, u32x4{a0, a1, a2, a3});
          ldsw<u32x4>(reg + 2 * TABS + 16 * m, u32x4{dl[rnd][1], dh[rnd][0], dh[rnd][1], dh[rnd][2]});
          ldsw<u32x4>(reg + 3 * TABS + 16 * m, u32x4{am, a0, a1, a2});
        }
        if (lane == 0) ldsw<float>(L_SYNC + 64 + 4 * aw, invs);
      }
      STAMP(3);
      lds_wait(L_SYNC + SY_TOK, 1);
      tag = rfl(lds_ld(L_SYNC + SY_TAG));
    }
    FENCE();
    lds_arrive(L_SYNC + SY_A);
    lds_wait(L_SYNC + SY_A, 4);
    STAMP(5);
    if (XP & 2) PRIO(3);
    {
      const u32 r = c16, g4 = q4;
      u32 selp[4];
      int lo7;
      {
        const int lam = 16 + (int)r - 8 * (int)g4;
        auto rne8 = [](int x) LAM { return (x + 3 + ((x >> 3) & 1)) >> 3; };
        lo7 = rne8(lam - 7);
#pragma unroll
        for (int i = 0; i < 4; ++i)
          selp[i] = (rne8(lam - 2 * i) > lo7 ? 0x0302u : 0x0100u) | (rne8(lam - 2 * i - 1) > lo7 ? 0x03020000u : 0x01000000u);
      }
      const u32 reg1 = (aw >> 1) * CHSZ, reg2 = 2 * CHSZ;
      const u32 dp1 = 8 * (aw & 1), dp2 = 4 * aw;
      const u32 fl = ((lane >> 2) & 3) * TABS + (32 - 8 * g4 + 4 * (lane & 3) - 4) * 2;
      const u32 pl = P3O + (lo7 - 128) * 4;
      const u32 gl = (16 * r - 16 + 8 * g4) * 2;
      const u32 f1 = reg1 + fl + 32 * dp1, f2 = reg2 + fl + 32 * dp2;
      const u32 p1 = reg1 + pl + 8 * dp1, p2 = reg2 + pl + 8 * dp2;
      const u32 u1 = reg1 + UPO + 512 + gl - 32 * dp1 - 192, u2 = reg2 + UPO + 1024 + gl - 512 * e - 32 * dp2 - 192;
      u32x4 F[2][8], G[2][8];
      u32 pv[2][3];
      auto ldz = [&](int i, int z) LAM {
        if (i > 5) return;
        const u32 st = i < 4 ? i : i - 4;
        if (z < 8) G[i & 1][z] = ldsr<u32x4>((i < 4 ? u1 : u2) + 192 - 64 * st + 512 * z);
        else if (z < 13) {
          const u32 ad = (i < 4 ? f1 : f2) + 64 * st + 512 * (z - 8);
          s16x4 x0 = ldstr(ad + 8), x1 = ldstr(ad);
          F[i & 1][z - 8] = __builtin_bit_cast(u32x4, __builtin_shufflevector(x0, x1, 0, 1, 2, 3, 4, 5, 6, 7));
        } else pv[i & 1][z - 13] = ldsr<u32>((i < 4 ? p1 : p2) + 16 * st + 128 * (z - 8));
      };
      auto prm = [&](int i, int m) LAM {
        if (i > 5) return;
#pragma unroll
        for (int q = 0; q < 4; ++q) F[i & 1][m][q] = __builtin_amdgcn_perm(pv[i & 1][m - 5], pv[i & 1][m - 5], selp[q]);
      };
      const int ord[16] = {8, 0, 1, 2, 3, 4, 5, 6, 7, 9, 10, 11, 12, 13, 14, 15};
      f32x4 y1[8] = {}, y2[4] = {};
#pragma unroll
      for (int z = 0; z < 16; ++z) ldz(0, ord[z]);
      prm(0, 5); prm(0, 6); prm(0, 7);
      sfor<0, 6>([&](auto I) LAM {
        constexpr int i = decltype(I)::v;
        int cnt = 0;
#pragma unroll
        for (int m = 0; m < 8; ++m)
#pragma unroll
          for (int T = 0; T < 8; ++T) {
            if (i < 4 ? T < m : (T >= 4 || T < (m >> 1))) continue;
            SB();
            if (i < 4) y1[T] = MF16H(F[i & 1][m], G[i & 1][T - m], y1[T]);
            else y2[T] = MF16H(F[i & 1][m], G[i & 1][2 * T + 1 - m], y2[T]);
            SB();
            if (i < 4) {
              if ((cnt & 1) == 0 && cnt / 2 < 16) ldz(i + 1, ord[cnt / 2]);
              if (cnt >= 33) prm(i + 1, cnt - 28);
            } else {
              if (cnt < 16) ldz(i + 1, ord[cnt]);
              if (cnt >= 17) prm(i + 1, cnt - 12);
            }
            ++cnt;
          }
        if (i == CDM - 1 || i == BDM - 1 || i == ADM - 1) {
          constexpr int ks0 = i == ADM - 1 ? 3 : i == BDM - 1 ? 4 : 5, ks1 = i == CDM - 1 ? 5 : i == BDM - 1 ? 4 : 3;
          lds_wait(L_SYNC + SY_G, 4 * (ks1 + 6));
          const int rot2 = ROT2;
          const u32 l16 = lane * 16;
#pragma unroll
          for (int ks = ks0; ks <= ks1; ++ks) {
            const char* g = a.ws + WS_W2R + (ct * 48 + 12 * aw + (ks + rot2 >= 12 ? ks + rot2 - 12 : ks + rot2)) * 5120;
            setm0(UNIT(aw, ks % 5));
            DMAC(l16, g, 0); DMAC(l16, g, 1024); DMAC(l16, g, 2048); DMAC(l16, g, 3072);
            setm0(UNIT(aw, ks % 5) + 4096);
            DMAC(l16, g + 4096, 0);
          }
        }
        if (i == DDM - 1 || i == EDM - 1) {
          constexpr int ks0 = i == DDM - 1 ? 6 : 7, ks1 = i == EDM - 1 ? 7 : 6;
          lds_wait(L_SYNC + SY_CS, 4);
          const int rot2 = ROT2;
          const u32 l16 = lane * 16;
#pragma unroll
          for (int ks = ks0; ks <= ks1; ++ks) {
            const char* g = a.ws + WS_W2R + (ct * 48 + 12 * aw + (ks + rot2 >= 12 ? ks + rot2 - 12 : ks + rot2)) * 5120;
            setm0(UNIT(aw, ks % 5));
            DMAC(l16, g, 0); DMAC(l16, g, 1024); DMAC(l16, g, 2048); DMAC(l16, g, 3072);
            setm0(UNIT(aw, ks % 5) + 4096);
            DMAC(l16, g + 4096, 0);
          }
        }
        if (i == UW) { WAITV(0); lds_arrive(L_SYNC + SY_U); }
      });
      SB();
      STAMP(6);
      if (XP & 1) PRIO(3);
      lds_arrive(L_SYNC + SY_T);
      const u32 hi = aw & 1;
      const u32 xa = reg1 + hi * 4096 + lane * 16, xo = reg1 + (hi ^ 1) * 4096;
      auto x2 = [](u32 s) LAM { return s < 2 ? s * CHSZ + 8192 : 2 * CHSZ + (s - 2) * 3072; };
      const float is1 = ldsr<float>(L_SYNC + 64 + 4 * (aw >> 1)), is2 = ldsr<float>(L_SYNC + 64 + 8);
      const u32 T2 = 2 * aw + 1 - e;
      const u32 to = (16 * r + 4 * g4) * 2;
      u32x2 uv[5];
#pragma unroll
      for (int i = 0; i < 5; ++i) uv[i] = ldsr<u32x2>((i < 4 ? reg1 + UPO + 512 + 512 * (4 * hi + i) : reg2 + UPO + 1024 + 512 * T2) + to);
      lds_spin(L_SYNC + SY_T, 4);
#pragma unroll
      for (int i = 0; i < 4; ++i) ldsw<f32x4>(xa + 1024 * i, selv4(hi, y1[i], y1[4 + i]));
#pragma unroll
      for (int i = 1; i < 4; ++i) {
        const int t = (aw + i) & 3;
        f32x4 v = selv4(t & 2, selv4(t & 1, y2[3], y2[2]), selv4(t & 1, y2[1], y2[0]));
        ldsw<f32x4>(x2(aw) + lane * 16 + 1024 * (i - 1), v);
      }
      FENCE();
      lds_arrive(L_SYNC + SY_A2);
      f32x4 yo[5];
#pragma unroll
      for (int i = 0; i < 4; ++i) yo[i] = selv4(hi, y1[4 + i], y1[i]);
      yo[4] = selv4(aw & 2, selv4(aw & 1, y2[3], y2[2]), selv4(aw & 1, y2[1], y2[0]));
      const float dk1 = bf_lo(dk1r) * 16.f, dk2 = bf_lo(dk2r) * 16.f;
      lds_spin(L_SYNC + SY_A2, 4);
#pragma unroll
      for (int i = 0; i < 4; ++i) yo[i] += ldsr<f32x4>(xo + lane * 16 + 1024 * i);
#pragma unroll
      for (int i = 1; i < 4; ++i) yo[4] += ldsr<f32x4>(x2((aw - i) & 3) + lane * 16 + 1024 * (i - 1));
      FENCE();
#pragma unroll
      for (int i = 0; i < 5; ++i) {
        const float is = i < 4 ? is1 : is2, dk = i < 4 ? dk1 : dk2;
        float sv[4];
#pragma unroll
        for (int q = 0; q < 4; ++q) {
          const u32 uq = uv[i][q >> 1];
          h16x2 hh = __builtin_bit_cast(h16x2, uq);
          sv[q] = __builtin_fmaf((float)hh[q & 1], dk, yo[i][q] * is);
        }
        ldsw<u32x2>(xo + 512 * i + to, u32x2{pk_bf(sv[0], sv[1]), pk_bf(sv[2], sv[3])});
      }
      FENCE();
      bst(ldsr<u32x4>(xo + 16 * lane), rw, sl1 * 4096 + hi * 2048 + 16 * lane, WS_ST);
      bst(ldsr<u32x4>(xo + 1024 + 16 * lane), rw, sl1 * 4096 + hi * 2048 + 1024 + 16 * lane, WS_ST);
      if (lane < 32) bst(ldsr<u32x4>(xo + 2048 + 16 * lane), rw, sl2 * 4096 + T2 * 512 + 16 * lane, WS_ST);
      STAMP(4);
      WAITV(0);
      WAITL0();
      if (lane == 0) __builtin_amdgcn_raw_buffer_store_b32(tag, rw, b * FSTR + 4 * aw, WS_F2, 16);
      lds_arrive(L_SYNC + SY_S);
    }
    STAMP(7);
    if (XP & 4) PRIO(0);
    {
      epi_setup();
      auto slot_of = [&](u32 dl) LAM { const u32 q = dl / 10; return (dl - 10 * q) * 64 + 8 * ct + q; };
      {
        const u32 sl = slot_of(20 * aw + umin(lane, 19)), px = sl / 80, pr = sl - 80 * px, pq = pr / 5, ps = pr - 5 * pq;
        const u32 pe = ps == 2 ? 1 - ((rt >> 2) & 1) : ps > 2;
        const u32 pw = ps == 2 ? rt >> 3 : 2 * (ps > 2 ? ps - 3 : ps) + (rt >> 4);
        const u32 fo = (px + 8 * (2 * pq + pe)) * FSTR + 4 * pw;
        lds_wait(L_SYNC + SY_W, F2N);
#pragma unroll 1
        for (int it = 0; it < SPIN_MAX; ++it) {
          u32 f = __builtin_amdgcn_raw_buffer_load_b32(rw, fo, WS_F2, 16);
          if (__builtin_amdgcn_ballot_w64(f != tag) == 0) break;
          __builtin_amdgcn_s_sleep(F2SL);
        }
        FENCE();
      }
      STAMP(8);
      u32x4 sv[3];
#pragma unroll
      for (int i = 0; i < 3; ++i) {
        const u32 id = umin(64 * i + lane, 159);
        sv[i] = bld(rw, slot_of(20 * aw + (id >> 3)) * 4096 + 128 * rt + 16 * (id & 7), WS_ST);
      }
      x_unpack();
      lds_wait(L_SYNC + SY_S, 4);
#pragma unroll
      for (int i = 0; i < 3; ++i) ldsw<u32x4>(L_S2 + 2560 * aw + 16 * umin(64 * i + lane, 159), sv[i]);
      WAITV(0);
    }
    STAMP(9);
  }
  WAITL0();
  __builtin_amdgcn_s_barrier();
  FENCE();
  STAMP(11);
  {
    auto fin = [&](int n, f32x4 v, s16x4 sq) LAM {
      const u32x2 sv = __builtin_bit_cast(u32x2, sq);
      float o[4];
#pragma unroll
      for (int q = 0; q < 4; ++q) {
        const float pre = v[q];
        const float g = pre * __builtin_amdgcn_rcpf(1.f + __builtin_amdgcn_exp2f(pre * -1.44269504089f));
        const u32 sw = sv[q >> 1];
        o[q] = __builtin_fmaf((q & 1) ? bf_hi(sw) : bf_lo(sw), g, xf[n][q]);
      }
      return u32x2{pk_bf(o[0], o[1]), pk_bf(o[2], o[3])};
    };
    const rsrc_t ro = __builtin_amdgcn_make_buffer_rsrc((void*)a.out, 0, 2048 * 1280, 0x00020000);
    if (wave >= 4) { own[0] = ldsr<f32x4>(ep0); own[1] = ldsr<f32x4>(ep0 + 1024); }
    f32x4 v[2];
    s16x4 sq[2];
#pragma unroll
    for (int n = 0; n < 2; ++n) {
      v[n] = (own[n] + ldsr<f32x4>(ep1 + 1024 * n)) + (ldsr<f32x4>(ep2 + 1024 * n) + ldsr<f32x4>(ep3 + 1024 * n));
      sq[n] = ldstr(es + 2048 * n);
    }
    if (wave < 4) {
      const f32x4 v2 = (own[2] + ldsr<f32x4>(ep1 + 2048)) + (ldsr<f32x4>(ep2 + 2048) + ldsr<f32x4>(ep3 + 2048));
      __builtin_amdgcn_raw_buffer_store_b64(fin(2, tr4(v2), ldstr(es + 4096)), ro, eo + 64, 0, OAUX);
    }
    const u32x2 r0 = fin(0, tr4(v[0]), sq[0]), r1 = fin(1, tr4(v[1]), sq[1]);
    if (OW) {
      auto x = __builtin_amdgcn_permlane16_swap(r0[0], r1[0], false, false);
      auto y = __builtin_amdgcn_permlane16_swap(r0[1], r1[1], false, false);
      __builtin_amdgcn_raw_buffer_store_b128(u32x4{x[0], y[0], x[1], y[1]}, ro, eo + 24 * (q4 & 1), 0, OAUX);
    } else {
      __builtin_amdgcn_raw_buffer_store_b64(r0, ro, eo, 0, OAUX);
      __builtin_amdgcn_raw_buffer_store_b64(r1, ro, eo + 32, 0, OAUX);
    }
  }
  asm volatile("s4_end:");
  STAMP_FLUSH();
}

extern "C" void kernel_launch(void* const* d_in, const int* in_sizes, int n_in, void* d_out, int out_size, void* d_ws,
                              size_t ws_size, hipStream_t stream) {
  Args a;
  a.x = (const u16*)d_in[0]; a.are = (const u16*)d_in[1]; a.aim = (const u16*)d_in[2]; a.bre = (const u16*)d_in[3];
  a.bim = (const u16*)d_in[4]; a.cre = (const u16*)d_in[5]; a.cim = (const u16*)d_in[6]; a.ldt = (const u16*)d_in[7];
  a.dsk = (const u16*)d_in[8]; a.w1 = (const u16*)d_in[10]; a.b1 = (const u16*)d_in[11]; a.w2 = (const u16*)d_in[12];
  a.b2 = (const u16*)d_in[13]; a.out = (u16*)d_out; a.ws = (char*)d_ws;
  (void)hipFuncSetAttribute((const void*)s4_block, hipFuncAttributeMaxDynamicSharedMemorySize, LDS_BYTES);
  hipLaunchKernelGGL(s4_block, dim3(256), dim3(512), LDS_BYTES, stream, a);
}
```
